```python
import torch

def _softmax(x, axis=-1):
    return torch.softmax(x, dim=axis)
def _gelu(x):
    return torch.nn.functional.gelu(x, approximate="tanh")
def _silu(x):
    return torch.nn.functional.silu(x)
def _sigmoid(x):
    return torch.sigmoid(x)
def _layernorm(x, axis=-1, eps=1e-5):
    m = x.mean(dim=axis, keepdim=True)
    v = x.var(dim=axis, keepdim=True, unbiased=False)
    return (x - m) / torch.sqrt(v + eps)
def _scatter_add(tgt, idx, src, axis=0):
    return tgt.clone().index_add_(axis, idx, src)
def _segment_sum(src, seg, n):
    out = torch.zeros((n,) + src.shape[1:], dtype=src.dtype)
    return out.index_add_(0, seg, src)
def _pad(x, pw):
    flat = tuple(v for pair in reversed(pw) for v in pair)
    return torch.nn.functional.pad(x, flat)

kernel_name = 'parallel_sum__gqa_block_cached__dense_activation_relu__decode'

def setup_inputs():

    _w = (lambda a: (lambda t: t.to(torch.bfloat16) if t.is_floating_point() else t)(torch.as_tensor(a)))
    def _A_setup():
        B, Sq, D = 1, 1, 448
        Hq, Hkv, Sk, Dh = 16, 8, 32768, 256
        assert Hq % Hkv == 0, (Hq, Hkv)
        return {
            "x": _w((lambda *s: torch.randn(*s, dtype=torch.bfloat16) * 0.05)(B, Sq, D)),
            "k_cache": _w((lambda *s: torch.randn(*s, dtype=torch.bfloat16) * 3.2)(B, Hkv, Sk, Dh)),
            "v_cache": _w((lambda *s: torch.randn(*s, dtype=torch.bfloat16) * 1.6)(B, Hkv, Sk, Dh)),
            "wq": _w((lambda *s: torch.randn(*s, dtype=torch.bfloat16) * 3.2)(D, Hq * Dh) * (D ** -0.5)),
            "wo": _w((lambda *s: torch.randn(*s, dtype=torch.bfloat16) * 1.6)(Hq * Dh, D) * ((Hq * Dh) ** -0.5)),
        }
    _A_in = _A_setup()

    def _B_setup():
        B, S, D, Dff = 1, 1, 448, 1792
        return {
            "x": _w((lambda *s: torch.randn(*s, dtype=torch.bfloat16) * 0.05)(B, S, D)),
            "w1": _w((lambda *s: torch.randn(*s, dtype=torch.bfloat16) * 0.05)(D, Dff)),
            "w2": _w((lambda *s: torch.randn(*s, dtype=torch.bfloat16) * 0.05)(Dff, D)),
        }
    _B_in = _B_setup()

    return {
        "A_x": _A_in["x"],
        "A_k_cache": _A_in["k_cache"],
        "A_v_cache": _A_in["v_cache"],
        "A_wq": _A_in["wq"],
        "A_wo": _A_in["wo"],
        "B_x": _B_in["x"],
        "B_w1": _B_in["w1"],
        "B_w2": _B_in["w2"],
    }

def reference(A_x, A_k_cache, A_v_cache, A_wq, A_wo, B_x, B_w1, B_w2):
    def _A_ref(x, k_cache, v_cache, wq, wo):
        B, Sq, D = x.shape
        Hq, Dh = 16, 256
        Hkv = k_cache.shape[1]
        rep = Hq // Hkv
        q = (lambda x, *d: x.permute(*d))((x @ wq).reshape(B, Sq, Hq, Dh), 0, 2, 1, 3)
        k = (lambda x, n, axis: torch.repeat_interleave(x, n, dim=axis))(k_cache, rep, axis=1)
        v = (lambda x, n, axis: torch.repeat_interleave(x, n, dim=axis))(v_cache, rep, axis=1)
        scores = torch.einsum("bhqd,bhkd->bhqk", q, k) * (Dh ** -0.5)
        p = _softmax(scores, axis=-1)
        o = torch.einsum("bhqk,bhkd->bhqd", p, v)
        return (lambda x, *d: x.permute(*d))(o, 0, 2, 1, 3).reshape(B, Sq, Hq * Dh) @ wo

    def _B_ref(x, w1, w2):
        h = torch.einsum("bsd,df->bsf", x, w1)
        zero = torch.zeros_like(h)
        h = torch.maximum(h, zero)
        return torch.einsum("bsf,fd->bsd", h, w2)

    x = A_x
    xn = _layernorm(x, axis=-1)
    return x + _A_ref(xn, A_k_cache, A_v_cache, A_wq, A_wo) + _B_ref(xn, B_w1, B_w2)
```

```cpp
#include <hip/hip_runtime.h>
#include <hip/hip_bf16.h>
#include <stdint.h>

#define ROUND_S 1

typedef unsigned u32x4 __attribute__((ext_vector_type(4)));
typedef float f32x4 __attribute__((ext_vector_type(4)));
typedef __bf16 bf16x2 __attribute__((ext_vector_type(2)));

constexpr int D = 448, HQ = 16, HKV = 8, SK = 32768, DH = 256, DFF = 1792;
constexpr int NSPLIT = 32, NWG = 256;
constexpr float EPS = 1e-5f;

__device__ __forceinline__ float bflo(unsigned u) { return __builtin_bit_cast(float, u << 16); }
__device__ __forceinline__ float bfhi(unsigned u) { return __builtin_bit_cast(float, u & 0xffff0000u); }
__device__ __forceinline__ float rbf(float f) {
  return (float)(__bf16)f;
}
__device__ __forceinline__ unsigned short tobf(float f) {
  return __builtin_bit_cast(unsigned short, (__bf16)f);
}
__device__ __forceinline__ float dot2(unsigned a, unsigned b, float c) {
  return __builtin_amdgcn_fdot2_f32_bf16(__builtin_bit_cast(bf16x2, a), __builtin_bit_cast(bf16x2, b), c, false);
}
template <int CTRL> __device__ __forceinline__ float dpp(float x) {
  return __builtin_bit_cast(float, __builtin_amdgcn_mov_dpp(__builtin_bit_cast(int, x), CTRL, 0xf, 0xf, true));
}
constexpr int XOR1 = 0xB1, XOR2 = 0x4E, XOR7 = 0x141, XOR8 = 0x128, ROR4 = 0x124;
__device__ __forceinline__ float wave_sum(float x) {
  x += dpp<XOR1>(x); x += dpp<XOR2>(x); x += dpp<XOR7>(x); x += dpp<XOR8>(x);
  auto s = __builtin_amdgcn_permlane16_swap(__float_as_uint(x), __float_as_uint(x), false, false);
  x = __uint_as_float(s[0]) + __uint_as_float(s[1]);
  auto t = __builtin_amdgcn_permlane32_swap(__float_as_uint(x), __float_as_uint(x), false, false);
  return __uint_as_float(t[0]) + __uint_as_float(t[1]);
}

__device__ __forceinline__ void layernorm_to_lds(const unsigned short* x, float* xn, int tid) {
  const int lane = tid & 63;
  u32x4 v = {0, 0, 0, 0};
  if (lane < 56) v = *reinterpret_cast<const u32x4*>(x + lane * 8);
  float f[8];
#pragma unroll
  for (int i = 0; i < 4; ++i) { f[2 * i] = bflo(v[i]); f[2 * i + 1] = bfhi(v[i]); }
  float s = 0.f;
#pragma unroll
  for (int i = 0; i < 8; ++i) s += f[i];
  s = wave_sum(s);
  const float mean = s * (1.0f / D);
  float ss = 0.f;
#pragma unroll
  for (int i = 0; i < 8; ++i) { const float d = f[i] - mean; ss += (lane < 56) ? d * d : 0.f; }
  ss = wave_sum(ss);
  const float mb = rbf(mean), vb = rbf(ss * (1.0f / D));
  const float den = rbf(sqrtf(rbf(vb + EPS)));
  if (tid < 56) {
#pragma unroll
    for (int i = 0; i < 8; ++i) xn[lane * 8 + i] = rbf(rbf(f[i] - mb) / den);
  }
}

__global__ void __launch_bounds__(256) k_proj(const unsigned short* x, const unsigned short* wq, const unsigned short* w1,
                                              unsigned short* q, float* h, float* oacc, unsigned* cnt) {
  __shared__ float xn[D];
  __shared__ float red[4][64];
  const int tid = threadIdx.x, b = blockIdx.x;
  if (b == 0) { for (int i = tid; i < D; i += 256) oacc[i] = 0.f; if (tid == 0) *cnt = 0u; }
  layernorm_to_lds(x, xn, tid);
  __syncthreads();
  const bool isq = b < 64;
  const unsigned short* w = isq ? wq + b * 64 : w1 + (b - 64) * 64;
  const int ld = isq ? HQ * DH : DFF;
  const int cg = tid & 7, rs = tid >> 3;
  u32x4 wv[14];
#pragma unroll
  for (int i = 0; i < 14; ++i) wv[i] = *reinterpret_cast<const u32x4*>(w + (size_t)(rs + 32 * i) * ld + cg * 8);
  float acc[8] = {0, 0, 0, 0, 0, 0, 0, 0};
#pragma unroll
  for (int i = 0; i < 14; ++i) { const float xr = xn[rs + 32 * i];
#pragma unroll
    for (int k = 0; k < 4; ++k) { acc[2 * k] = fmaf(xr, bflo(wv[i][k]), acc[2 * k]); acc[2 * k + 1] = fmaf(xr, bfhi(wv[i][k]), acc[2 * k + 1]); } }
#pragma unroll
  for (int k = 0; k < 8; ++k) { float a = acc[k]; a += __shfl_xor(a, 8); a += __shfl_xor(a, 16); a += __shfl_xor(a, 32); acc[k] = a; }
  if ((tid & 63) < 8) {
#pragma unroll
    for (int k = 0; k < 8; ++k) red[tid >> 6][cg * 8 + k] = acc[k]; }
  __syncthreads();
  if (tid < 64) { const float r = red[0][tid] + red[1][tid] + red[2][tid] + red[3][tid];
    if (isq) q[b * 64 + tid] = tobf(r); else h[(b - 64) * 64 + tid] = fmaxf(rbf(r), 0.f); }
}

__global__ void __launch_bounds__(512, 2) k_attn(const unsigned short* kc, const unsigned short* vc, const unsigned short* q, float* part) {
  __shared__ float pl[8][2][64][2];
  __shared__ float mrg[8][2][260];
  const int tid = threadIdx.x, lane = tid & 63, w = __builtin_amdgcn_readfirstlane(tid >> 6);
  const int g = blockIdx.x & 7, s = blockIdx.x >> 3, hf = lane >> 5, c = lane & 31;
  const u32x4 q0 = *reinterpret_cast<const u32x4*>(q + (2 * g) * DH + 8 * c);
  const u32x4 q1 = *reinterpret_cast<const u32x4*>(q + (2 * g + 1) * DH + 8 * c);
  const size_t key0 = (size_t)g * SK + s * 1024 + w * 128;
  const u32x4* kp = reinterpret_cast<const u32x4*>(kc + key0 * DH) + lane;
  const u32x4* vp = reinterpret_cast<const u32x4*>(vc + key0 * DH) + lane;
  float sc[4];
#pragma unroll 1
  for (int ch = 0; ch < 4; ++ch) {
    u32x4 kv[16];
#pragma unroll
    for (int i = 0; i < 16; ++i) kv[i] = __builtin_nontemporal_load(kp + (ch * 16 + i) * 64);
    float x[32];
#pragma unroll
    for (int i = 0; i < 16; ++i) { float a = 0.f, b = 0.f;
#pragma unroll
      for (int k = 0; k < 4; ++k) { a = dot2(kv[i][k], q0[k], a); b = dot2(kv[i][k], q1[k], b); }
      x[2 * i] = a; x[2 * i + 1] = b; }
#pragma unroll
    for (int hh = 0; hh < 16; ++hh) {
      auto r = __builtin_amdgcn_permlane16_swap(__float_as_uint(x[hh]), __float_as_uint(x[hh + 16]), false, false);
      x[hh] = __uint_as_float(r[0]) + __uint_as_float(r[1]); }
#define TR(HALF, CTRL, BIT) _Pragma("unroll") for (int hh = 0; hh < (HALF); ++hh) { \
      const float keep = (c & (BIT)) ? x[hh + (HALF)] : x[hh], send = (c & (BIT)) ? x[hh] : x[hh + (HALF)]; x[hh] = keep + dpp<CTRL>(send); }
    TR(8, XOR8, 8) TR(4, XOR7, 4) TR(2, XOR2, 2) TR(1, XOR1, 1)
#undef TR
    float sv = x[0];
    if (ROUND_S) sv = rbf(sv);
    const float sval = sv * 0.0625f;
    sc[0] = ch == 0 ? sval : sc[0]; sc[1] = ch == 1 ? sval : sc[1]; sc[2] = ch == 2 ? sval : sc[2]; sc[3] = ch == 3 ? sval : sc[3];
  }
  float m = fmaxf(fmaxf(sc[0], sc[1]), fmaxf(sc[2], sc[3]));
  m = fmaxf(m, dpp<XOR2>(m)); m = fmaxf(m, dpp<ROR4>(m)); m = fmaxf(m, dpp<XOR8>(m));
  { auto r = __builtin_amdgcn_permlane16_swap(__float_as_uint(m), __float_as_uint(m), false, false); m = fmaxf(__uint_as_float(r[0]), __uint_as_float(r[1]));
    auto t = __builtin_amdgcn_permlane32_swap(__float_as_uint(m), __float_as_uint(m), false, false); m = fmaxf(__uint_as_float(t[0]), __uint_as_float(t[1])); }
  float l = 0.f;
#pragma unroll
  for (int ch = 0; ch < 4; ++ch) { const float p = __expf(sc[ch] - m); l += p; pl[w][hf][ch * 16 + (c >> 1)][c & 1] = p; }
  l += dpp<XOR2>(l); l += dpp<ROR4>(l); l += dpp<XOR8>(l);
  { auto r = __builtin_amdgcn_permlane16_swap(__float_as_uint(l), __float_as_uint(l), false, false); l = __uint_as_float(r[0]) + __uint_as_float(r[1]);
    auto t = __builtin_amdgcn_permlane32_swap(__float_as_uint(l), __float_as_uint(l), false, false); l = __uint_as_float(t[0]) + __uint_as_float(t[1]); }
  asm volatile("s_waitcnt lgkmcnt(0)" ::: "memory");
  float acc[16];
#pragma unroll
  for (int k = 0; k < 16; ++k) acc[k] = 0.f;
#pragma unroll 1
  for (int ch = 0; ch < 4; ++ch) {
    u32x4 vv[16];
#pragma unroll
    for (int i = 0; i < 16; ++i) vv[i] = __builtin_nontemporal_load(vp + (ch * 16 + i) * 64);
#pragma unroll
    for (int i = 0; i < 16; i += 2) {
      const f32x4 pp = *reinterpret_cast<const f32x4*>(&pl[w][hf][ch * 16 + i][0]);
#pragma unroll
      for (int u = 0; u < 2; ++u)
#pragma unroll
        for (int k = 0; k < 4; ++k) { const float a = bflo(vv[i + u][k]), b = bfhi(vv[i + u][k]);
          acc[2 * k] = fmaf(pp[2 * u], a, acc[2 * k]); acc[2 * k + 1] = fmaf(pp[2 * u], b, acc[2 * k + 1]);
          acc[8 + 2 * k] = fmaf(pp[2 * u + 1], a, acc[8 + 2 * k]); acc[8 + 2 * k + 1] = fmaf(pp[2 * u + 1], b, acc[8 + 2 * k + 1]); }
    }
  }
#pragma unroll
  for (int k = 0; k < 16; ++k) { auto t = __builtin_amdgcn_permlane32_swap(__float_as_uint(acc[k]), __float_as_uint(acc[k]), false, false);
    acc[k] = __uint_as_float(t[0]) + __uint_as_float(t[1]); }
  { float* rec = &mrg[w][hf][0];
    f32x4 a = {hf ? acc[8] : acc[0], hf ? acc[9] : acc[1], hf ? acc[10] : acc[2], hf ? acc[11] : acc[3]};
    f32x4 b = {hf ? acc[12] : acc[4], hf ? acc[13] : acc[5], hf ? acc[14] : acc[6], hf ? acc[15] : acc[7]};
    *reinterpret_cast<f32x4*>(rec + 4 + 8 * c) = a; *reinterpret_cast<f32x4*>(rec + 8 + 8 * c) = b;
    if (lane < 2) { mrg[w][lane][0] = m; mrg[w][lane][1] = l; } }
  __syncthreads();
  { const int h = tid >> 8, d = tid & 255;
    float M = mrg[0][h][0];
#pragma unroll
    for (int ww = 1; ww < 8; ++ww) M = fmaxf(M, mrg[ww][h][0]);
    float L = 0.f, o = 0.f;
#pragma unroll
    for (int ww = 0; ww < 8; ++ww) { const float wt = __expf(mrg[ww][h][0] - M); L = fmaf(wt, mrg[ww][h][1], L); o = fmaf(wt, mrg[ww][h][4 + d], o); }
    float* dst = part + ((size_t)(g * 2 + h) * NSPLIT + s) * 260;
    dst[4 + d] = o;
    if (d == 0) { dst[0] = M; dst[1] = L; } }
}

__global__ void __launch_bounds__(512) k_tail(const float* part, const float* h, const unsigned short* wo, const unsigned short* w2,
                                              const unsigned short* x, float* oacc, unsigned* cnt, unsigned short* out) {
  __shared__ float ov[32];
  __shared__ float red[16][32];
  __shared__ unsigned last;
  const int tid = threadIdx.x, b = blockIdx.x, g = b & 7, s = b >> 3;
  const int hd = 2 * g + (s >> 4), d0 = (s & 15) * 16;
  { const int src = tid >> 4, d = tid & 15;
    const float* rec = part + ((size_t)hd * NSPLIT + src) * 260;
    const float mv = rec[0], lv = rec[1], o = rec[4 + d0 + d];
    float M = mv;
#pragma unroll
    for (int k = 16; k < 64; k <<= 1) M = fmaxf(M, __shfl_xor(M, k));
    red[0][tid >> 6] = M;
    __syncthreads();
    M = red[0][0];
#pragma unroll
    for (int k = 1; k < 8; ++k) M = fmaxf(M, red[0][k]);
    __syncthreads();
    const float wt = __expf(mv - M);
    float L = wt * lv, oo = wt * o;
    L += __shfl_xor(L, 16); L += __shfl_xor(L, 32); oo += __shfl_xor(oo, 16); oo += __shfl_xor(oo, 32);
    if ((tid & 63) < 16) { red[tid >> 6][d] = oo; red[tid >> 6][16 + d] = L; }
    __syncthreads();
    if (tid < 16) { float O = 0.f, LL = 0.f;
#pragma unroll
      for (int k = 0; k < 8; ++k) { O += red[k][tid]; LL += red[k][16 + tid]; }
      ov[tid] = rbf(O / LL); }
    if (tid >= 16 && tid < 23) ov[tid] = h[b * 7 + tid - 16];
    __syncthreads(); }
  if (tid < D) {
    float a = 0.f, f = 0.f;
#pragma unroll
    for (int r = 0; r < 16; ++r) a = fmaf(ov[r], __builtin_bit_cast(float, (unsigned)wo[(size_t)(hd * DH + d0 + r) * D + tid] << 16), a);
#pragma unroll
    for (int r = 0; r < 7; ++r) f = fmaf(ov[16 + r], __builtin_bit_cast(float, (unsigned)w2[(size_t)(b * 7 + r) * D + tid] << 16), f);
    atomicAdd(oacc + tid, a + f);
  }
  asm volatile("s_waitcnt vmcnt(0)" ::: "memory");
  __syncthreads();
  if (tid == 0) { const unsigned old = __hip_atomic_fetch_add(cnt, 1u, __ATOMIC_RELAXED, __HIP_MEMORY_SCOPE_AGENT); last = old == NWG - 1;
    if (last) { __builtin_amdgcn_fence(__ATOMIC_ACQUIRE, "agent"); asm volatile("s_waitcnt vmcnt(0)" ::: "memory"); } }
  __syncthreads();
  if (last && tid < D) {
    const float a = __hip_atomic_load(oacc + tid, __ATOMIC_RELAXED, __HIP_MEMORY_SCOPE_AGENT);
    const float xv = __builtin_bit_cast(float, (unsigned)x[tid] << 16);
    out[tid] = tobf(xv + a);
  }
}

extern "C" void kernel_launch(void* const* d_in, const int* in_sizes, int n_in, void* d_out, int out_size, void* d_ws, size_t ws_size, hipStream_t stream) {
  const unsigned short* x = (const unsigned short*)d_in[0];
  const unsigned short* kc = (const unsigned short*)d_in[1];
  const unsigned short* vc = (const unsigned short*)d_in[2];
  const unsigned short* wq = (const unsigned short*)d_in[3];
  const unsigned short* wo = (const unsigned short*)d_in[4];
  const unsigned short* w1 = (const unsigned short*)d_in[6];
  const unsigned short* w2 = (const unsigned short*)d_in[7];
  char* ws = (char*)d_ws;
  unsigned* cnt = (unsigned*)ws;
  float* oacc = (float*)(ws + 256);
  unsigned short* q = (unsigned short*)(ws + 4096);
  float* h = (float*)(ws + 16384);
  float* part = (float*)(ws + 32768);
  k_proj<<<92, 256, 0, stream>>>(x, wq, w1, q, h, oacc, cnt);
  k_attn<<<NWG, 512, 0, stream>>>(kc, vc, q, part);
  k_tail<<<NWG, 512, 0, stream>>>(part, h, wo, w2, x, oacc, cnt, (unsigned short*)d_out);
}
```

```cpp
#include <hip/hip_runtime.h>
#include <stdint.h>

typedef unsigned u32x4 __attribute__((ext_vector_type(4)));
typedef unsigned u32x2 __attribute__((ext_vector_type(2)));
typedef float f32x4 __attribute__((ext_vector_type(4)));
typedef float f32x2 __attribute__((ext_vector_type(2)));
typedef __bf16 bf16x2 __attribute__((ext_vector_type(2)));
#define LAS __attribute__((address_space(3)))
#define GAS __attribute__((address_space(1)))

#define P0 6
#define ROT 1
#define WAUX 2
#define STAMPS 0
#define NOQ 0
#define NOCK 0
#define NOCV 0
#define NOW_ 0
#define SW 0
#define DRAIN 0
#define QSLEEP 2
#define MLSLEEP 16
#define NOML 0
#define AUXQ WAUX
#define AUX1 WAUX
#define TAU 3790
#define TOFF 280
#define PG 4
#define DRAIN1 1
#define DRAIN2 1
#define DELAY1 0
#define DELAY2 0
#define SAUX 16
#define SLEEP1 1

constexpr int D = 448, SK = 32768, DH = 256, DFF = 1792, NQ = 4096;
#define WIN 16
constexpr int NWG = 256, NTHR = 512, W = WIN, NIT = 128 / W, NG = W / 16;
constexpr float EPS = 1e-5f;
constexpr unsigned SPIN_CAP = 1u << 17;
constexpr unsigned WS_EPOCH = 0, WS_ML = 4096, WS_XQ = 16384, WS_XH = WS_XQ + 131072, WS_X1 = 524288, WS_X2 = WS_X1 + (1u << 20), WS_STAMP = 3u << 20, WS_USED = 4u << 20;
static_assert(WS_ML + 8192 <= WS_XQ && WS_XH + 224 * 512 <= WS_X1 && WS_X2 + 256 * 3584 <= WS_STAMP, "ws");
constexpr unsigned L_Q = 0;
constexpr unsigned L_XN = 1024;
constexpr unsigned L_QP = L_XN + 4 * 448 * 4;
constexpr unsigned L_CNT = L_QP + 1024;
constexpr unsigned L_MLW = L_CNT + 16;
constexpr unsigned L_FAC = L_MLW + 64;
constexpr unsigned L_FFN = L_FAC + 32;
constexpr unsigned L_FIN = L_FFN + 448 * 4;
constexpr unsigned L_P = L_FIN + 128;
constexpr unsigned L_ACC = L_P + 8192;
constexpr unsigned L_END = L_ACC + 8192;
constexpr unsigned LDS_BYTES = 88 * 1024;
static_assert(L_END <= LDS_BYTES && L_P % 16 == 0 && L_FFN % 16 == 0 && L_FAC % 16 == 0 && L_MLW % 16 == 0, "lds");

__device__ __forceinline__ float bflo(unsigned u) { return __builtin_bit_cast(float, u << 16); }
__device__ __forceinline__ float bfhi(unsigned u) { return __builtin_bit_cast(float, u & 0xffff0000u); }
__device__ __forceinline__ float rbf(float f) { return (float)(__bf16)f; }
__device__ __forceinline__ unsigned pk2(float lo, float hi) { bf16x2 v = {(__bf16)lo, (__bf16)hi}; return __builtin_bit_cast(unsigned, v); }
__device__ __forceinline__ float dot2(unsigned a, unsigned b, float c) {
  return __builtin_amdgcn_fdot2_f32_bf16(__builtin_bit_cast(bf16x2, a), __builtin_bit_cast(bf16x2, b), c, false);
}
template <int CTRL> __device__ __forceinline__ float dpp(float x) {
  return __builtin_bit_cast(float, __builtin_amdgcn_mov_dpp(__builtin_bit_cast(int, x), CTRL, 0xf, 0xf, true));
}
constexpr int XOR1 = 0xB1, XOR2 = 0x4E, XOR3 = 0x1B, XOR7 = 0x141, XOR8 = 0x128, ROR4 = 0x124;
__device__ __forceinline__ float xor4(float x) { return dpp<XOR3>(dpp<XOR7>(x)); }
__device__ __forceinline__ float sw16_sum(float a, float b) {
  auto r = __builtin_amdgcn_permlane16_swap(__float_as_uint(a), __float_as_uint(b), false, false);
  return __uint_as_float(r[0]) + __uint_as_float(r[1]);
}
__device__ __forceinline__ float sw32_sum(float a, float b) {
  auto r = __builtin_amdgcn_permlane32_swap(__float_as_uint(a), __float_as_uint(b), false, false);
  return __uint_as_float(r[0]) + __uint_as_float(r[1]);
}
__device__ __forceinline__ float sw16_max(float a) {
  auto r = __builtin_amdgcn_permlane16_swap(__float_as_uint(a), __float_as_uint(a), false, false);
  return fmaxf(__uint_as_float(r[0]), __uint_as_float(r[1]));
}
__device__ __forceinline__ float sw32_max(float a) {
  auto r = __builtin_amdgcn_permlane32_swap(__float_as_uint(a), __float_as_uint(a), false, false);
  return fmaxf(__uint_as_float(r[0]), __uint_as_float(r[1]));
}
__device__ __forceinline__ float rdlane(float v, int l) { return __builtin_bit_cast(float, __builtin_amdgcn_readlane(__builtin_bit_cast(int, v), l)); }
#define BARRIER() do { __builtin_amdgcn_sched_barrier(0); asm volatile("s_waitcnt lgkmcnt(0)\n\ts_barrier" ::: "memory"); __builtin_amdgcn_sched_barrier(0); } while (0)
#define NOW() __builtin_amdgcn_s_memrealtime()
__device__ __forceinline__ void pace(unsigned tgt) {
  asm volatile("pace_%=:\n\ts_memrealtime s[60:61]\n\ts_waitcnt lgkmcnt(0)\n\ts_sub_u32 s60, s60, %0\n\ts_cmp_lt_i32 s60, 0\n\ts_cbranch_scc1 pace_%=" :: "s"(tgt) : "memory", "scc", "s60", "s61");
}
__device__ __forceinline__ unsigned lds_ld(LAS unsigned* p) { return __hip_atomic_load(p, __ATOMIC_RELAXED, __HIP_MEMORY_SCOPE_WORKGROUP); }
__device__ __forceinline__ void lds_inc(LAS unsigned* p) { (void)__hip_atomic_fetch_add(p, 1u, __ATOMIC_RELAXED, __HIP_MEMORY_SCOPE_WORKGROUP); }
__device__ __forceinline__ bool tag_ok(const u32x4& v, unsigned tag) { return v[0] == tag && v[2] == tag; }

struct Args {
  const unsigned short *x, *kc, *vc, *wq, *wo, *w1, *w2;
  unsigned short* out;
  unsigned char* ws;
};

__global__ void __launch_bounds__(NTHR, 2) fused_decode(Args a) {
  extern __shared__ __attribute__((aligned(16))) unsigned char smem[];
  LAS unsigned char* L = (LAS unsigned char*)smem;
  const int tid = threadIdx.x, lane = tid & 63, w = __builtin_amdgcn_readfirstlane(tid >> 6);
  const int b = blockIdx.x, g = b & 7, s = b >> 3;
#define STAMP(i)

  if (w < 4) {
    const int hf = lane >> 5, c = lane & 31;
    const unsigned voff = lane * 16;
    const unsigned sbase = (unsigned)s * (1024u * 512u) + (unsigned)w * 2048u;
    const int rp = (s * ROT) & 63;
    const unsigned short* kh = a.kc + (size_t)g * SK * DH;
    const unsigned short* vh = a.vc + (size_t)g * SK * DH;
    constexpr int HEADB = SK * DH * 2;
    auto soff = [&](int p) -> unsigned { return sbase + ((unsigned)((p + rp) & 63) << 13); };
    u32x4 buf[W];
    const unsigned tb = (unsigned)NOW() + TOFF;
    BARRIER();
    {
      const __amdgpu_buffer_rsrc_t rk = __builtin_amdgcn_make_buffer_rsrc((void*)kh, 0, HEADB, 0x00020000);
#pragma unroll
      for (int i = 0; i < W; ++i) {
        if (i >= P0) asm volatile("s_waitcnt vmcnt(%0)" ::"i"(P0 - 1) : "memory");
        buf[i] = __builtin_amdgcn_raw_buffer_load_b128(rk, voff + (i & 1) * 1024, soff(i >> 1), 2);
        __builtin_amdgcn_sched_barrier(0);
      }
    }
    BARRIER();
    const u32x4 q0 = *(const LAS u32x4*)(L + L_Q + c * 16), q1 = *(const LAS u32x4*)(L + L_Q + 512 + c * 16);
    LAS float* pl = (LAS float*)(L + L_P) + w * 512 + hf * 256;
    float m = -INFINITY;
#pragma unroll 1
    for (int it = 0; it < NIT; ++it) {
      const bool tok = it < NIT - 1;
      const __amdgpu_buffer_rsrc_t rn = __builtin_amdgcn_make_buffer_rsrc((void*)(tok ? kh : vh), 0, HEADB, 0x00020000);
      const int pb = tok ? (it + 1) * (W / 2) : 0;
#pragma unroll
      for (int gr = 0; gr < NG; ++gr) {
        float x[32];
#pragma unroll
        for (int i = 0; i < 16; ++i) {
          const int idx = gr * 16 + i;
          float s0 = 0.f, s1 = 0.f;
#pragma unroll
          for (int k = 0; k < 4; ++k) { s0 = dot2(buf[idx][k], q0[k], s0); s1 = dot2(buf[idx][k], q1[k], s1); }
          x[2 * i] = s0; x[2 * i + 1] = s1;
          if (TAU && (idx % PG) == 0) { asm volatile("" : "+v"(x[2 * i]), "+v"(x[2 * i + 1]) :: "memory"); pace(tb + (((unsigned)(it * W + idx) * TAU) >> 8)); }
          buf[idx] = __builtin_amdgcn_raw_buffer_load_b128(rn, voff + (idx & 1) * 1024, soff(pb + (idx >> 1)), 2);
          __builtin_amdgcn_sched_barrier(0);
        }
#pragma unroll
        for (int hh = 0; hh < 16; ++hh) x[hh] = sw16_sum(x[hh], x[hh + 16]);
#define TR(HALF, CTRL, BIT) _Pragma("unroll") for (int hh = 0; hh < (HALF); ++hh) { \
          const float keep = (c & (BIT)) ? x[hh + (HALF)] : x[hh], send = (c & (BIT)) ? x[hh] : x[hh + (HALF)]; x[hh] = keep + dpp<CTRL>(send); }
        TR(8, XOR8, 8) TR(4, XOR7, 4) TR(2, XOR2, 2) TR(1, XOR1, 1)
#undef TR
        const float sv = rbf(x[0]) * 0.0625f;
        m = fmaxf(m, sv);
        pl[(it * NG + gr) * 32 + c] = sv;
        __builtin_amdgcn_sched_barrier(0);
      }
    }
    STAMP(4);
    m = fmaxf(m, dpp<XOR2>(m)); m = fmaxf(m, dpp<ROR4>(m)); m = fmaxf(m, dpp<XOR8>(m)); m = sw16_max(m); m = sw32_max(m);
    float l = 0.f;
    {
      float sc[8];
#pragma unroll
      for (int j = 0; j < 8; ++j) sc[j] = pl[j * 32 + c];
#pragma unroll
      for (int j = 0; j < 8; ++j) { const float p = __expf(sc[j] - m); l += p; pl[j * 32 + c] = p; }
    }
    l += dpp<XOR2>(l); l += dpp<ROR4>(l); l += dpp<XOR8>(l); l = sw16_sum(l, l); l = sw32_sum(l, l);
    if (lane < 2) *(LAS f32x2*)(L + L_MLW + (w * 2 + lane) * 8) = f32x2{m, l};
    if (lane == 0) lds_inc((LAS unsigned*)(L + L_CNT) + 1);
    asm volatile("s_waitcnt lgkmcnt(0)" ::: "memory");
    f32x2 ac2[8];
#pragma unroll
    for (int k = 0; k < 8; ++k) ac2[k] = f32x2{0.f, 0.f};
#pragma unroll 1
    for (int it = 0; it < NIT; ++it) {
      const __amdgpu_buffer_rsrc_t rn = __builtin_amdgcn_make_buffer_rsrc((void*)vh, 0, it < NIT - 1 ? HEADB : 0, 0x00020000);
      const int pb = (it + 1) * (W / 2);
      const unsigned tv = tb + ((((unsigned)(128 + it * W) * TAU) >> 8) & (it < NIT - 1 ? ~0u : 0u));
      f32x4 pp = *(const LAS f32x4*)(pl + it * W * 2);
#pragma unroll
      for (int i = 0; i < W; i += 2) {
        const f32x4 pc = pp;
        if (i + 2 < W) pp = *(const LAS f32x4*)(pl + (it * W + i + 2) * 2);
#pragma unroll
        for (int u = 0; u < 2; ++u) {
          const f32x2 pa = {pc[2 * u], pc[2 * u]}, pb2 = {pc[2 * u + 1], pc[2 * u + 1]};
#pragma unroll
          for (int k = 0; k < (NOCV ? 1 : 4); ++k) {
            const f32x2 vv = NOCV ? f32x2{__uint_as_float(buf[i + u][0] ^ buf[i + u][1]), __uint_as_float(buf[i + u][2] ^ buf[i + u][3])} : f32x2{bflo(buf[i + u][k]), bfhi(buf[i + u][k])};
            ac2[k] = __builtin_elementwise_fma(pa, vv, ac2[k]);
            ac2[4 + k] = __builtin_elementwise_fma(pb2, vv, ac2[4 + k]);
          }
          asm volatile("" : "+v"(ac2[0]), "+v"(ac2[1]), "+v"(ac2[2]), "+v"(ac2[3]), "+v"(ac2[4]), "+v"(ac2[5]), "+v"(ac2[6]), "+v"(ac2[7]) :: "memory");
          if (TAU && ((i + u) % PG) == 0) pace(tv + (((unsigned)(i + u) * TAU) >> 8));
          buf[i + u] = __builtin_amdgcn_raw_buffer_load_b128(rn, voff + u * 1024, soff(pb + (i >> 1)), 2);
          __builtin_amdgcn_sched_barrier(0);
        }
      }
    }
    STAMP(5);
    {
      float acc[8];
#pragma unroll
      for (int k = 0; k < 4; ++k) { acc[2 * k] = sw32_sum(ac2[k][0], ac2[4 + k][0]); acc[2 * k + 1] = sw32_sum(ac2[k][1], ac2[4 + k][1]); }
      LAS float* rec = (LAS float*)(L + L_ACC) + (w * 2 + hf) * 256 + 8 * c;
      *(LAS f32x4*)rec = f32x4{acc[0], acc[1], acc[2], acc[3]};
      *(LAS f32x4*)(rec + 4) = f32x4{acc[4], acc[5], acc[6], acc[7]};
    }
    BARRIER();
    return;
  }

  STAMP(0);
  const int hw = w - 4, ht = tid - 256;
  const __amdgpu_buffer_rsrc_t rws = __builtin_amdgcn_make_buffer_rsrc((void*)a.ws, 0, WS_USED, 0x00020000);
  LAS unsigned* cnt = (LAS unsigned*)(L + L_CNT);
  LAS float* xn = (LAS float*)(L + L_XN) + hw * 448;
  unsigned tag;
  {
    const int cb = s >> 2, kq = s & 3;
    const __amdgpu_buffer_rsrc_t rx = __builtin_amdgcn_make_buffer_rsrc((void*)a.x, 0, D * 2, 0x00020000);
    const __amdgpu_buffer_rsrc_t rq = __builtin_amdgcn_make_buffer_rsrc((void*)(a.wq + (size_t)kq * 112 * NQ), 0, (NOW_ & 1) ? 0 : 112 * NQ * 2, 0x00020000);
    const u32x4 xv = __builtin_amdgcn_raw_buffer_load_b128(rx, lane * 16, 0, 0);
    const int cg = ht & 7, rs = ht >> 3;
    u32x4 wv[4];
    const unsigned qo = (unsigned)rs * (NQ * 2) + (unsigned)(g * 512 + cb * 64 + cg * 8) * 2;
#pragma unroll
    for (int i = 0; i < 4; ++i) wv[i] = __builtin_amdgcn_raw_buffer_load_b128(rq, qo + i * 32 * (NQ * 2), 0, WAUX);
    const unsigned ep = __builtin_amdgcn_raw_buffer_load_b32(rws, 0, WS_EPOCH, 16);
    if (ht < 2) cnt[ht] = 0u;
    BARRIER();
    STAMP(12);
    float f[8];
#pragma unroll
    for (int i = 0; i < 4; ++i) { f[2 * i] = bflo(xv[i]); f[2 * i + 1] = bfhi(xv[i]); }
    const float x0 = rdlane(f[0], 0);
    float s1 = 0.f, s2 = 0.f;
#pragma unroll
    for (int i = 0; i < 8; ++i) { const float d = f[i] - x0; s1 += d; s2 = fmaf(d, d, s2); }
    if (lane >= 56) { s1 = 0.f; s2 = 0.f; }
    s1 += dpp<XOR1>(s1); s2 += dpp<XOR1>(s2); s1 += dpp<XOR2>(s1); s2 += dpp<XOR2>(s2);
    s1 += dpp<XOR7>(s1); s2 += dpp<XOR7>(s2); s1 += dpp<XOR8>(s1); s2 += dpp<XOR8>(s2);
    { const float t = sw16_sum(s1, s2);
      const float t2 = sw32_sum(t, t); s1 = rdlane(t2, 0); s2 = rdlane(t2, 16); }
    const float md = s1 * (1.0f / D);
    const float var = fmaxf(fmaf(-md, md, s2 * (1.0f / D)), 0.f);
    const float mb = rbf(x0 + md), den = rbf(sqrtf(rbf(rbf(var) + EPS)));
    const float rden = 1.0f / den;
    if (lane < 56) {
      f32x4 n0, n1;
#pragma unroll
      for (int i = 0; i < 4; ++i) { n0[i] = rbf(rbf(f[i] - mb) * rden); n1[i] = rbf(rbf(f[4 + i] - mb) * rden); }
      *(LAS f32x4*)(xn + lane * 8) = n0; *(LAS f32x4*)(xn + lane * 8 + 4) = n1;
    }
    asm volatile("" ::: "memory");
    STAMP(14);
    float xr[4];
#pragma unroll
    for (int i = 0; i < 4; ++i) { const int row = rs + 32 * i; xr[i] = xn[kq * 112 + (row < 112 ? row : 0)]; }
    float qa[8];
#pragma unroll
    for (int k = 0; k < 8; ++k) qa[k] = 0.f;
#pragma unroll
    for (int i = 0; i < 4; ++i) {
#pragma unroll
      for (int k = 0; k < 4; ++k) { qa[2 * k] = fmaf(xr[i], bflo(wv[i][k]), qa[2 * k]); qa[2 * k + 1] = fmaf(xr[i], bfhi(wv[i][k]), qa[2 * k + 1]); }
    }
#pragma unroll
    for (int k = 0; k < 4; ++k) qa[k] = sw32_sum(qa[k], qa[k + 4]);
#pragma unroll
    for (int k = 0; k < 2; ++k) qa[k] = sw16_sum(qa[k], qa[k + 2]);
    { const float keep = (lane & 8) ? qa[1] : qa[0], send = (lane & 8) ? qa[0] : qa[1]; qa[0] = keep + dpp<XOR8>(send); }
    LAS float* qp = (LAS float*)(L + L_QP);
    qp[hw * 64 + cg * 8 + (lane >> 5) * 4 + ((lane >> 4) & 1) * 2 + ((lane >> 3) & 1)] = qa[0];
    if (lane == 0) lds_inc(cnt);
    STAMP(1);
    tag = __builtin_amdgcn_readfirstlane(ep) + 1u;
    for (unsigned spin = 0; spin < SPIN_CAP; ++spin) { if (__builtin_amdgcn_readfirstlane(lds_ld(cnt)) == 4u) break; }
    asm volatile("" ::: "memory");
    const int pr = ht & 31, cbt = ht >> 5;
    f32x2 own = *(const LAS f32x2*)(qp + 2 * pr);
#pragma unroll
    for (int k = 1; k < 4; ++k) own += *(const LAS f32x2*)(qp + k * 64 + 2 * pr);
    if (cbt == 2 * hw && (lane >> 3) == hw)
      __builtin_amdgcn_raw_buffer_store_b128(u32x4{tag, __float_as_uint(own[0]), tag, __float_as_uint(own[1])}, rws, WS_XQ + ((g * 8 + cb) * 4 + kq) * 512 + pr * 16, 0, 16);
    STAMP(2);
    u32x4 gq[4]; bool ok[4];
#pragma unroll
    for (int k = 0; k < 4; ++k) { ok[k] = cbt == cb && k == kq; gq[k] = u32x4{tag, __float_as_uint(own[0]), tag, __float_as_uint(own[1])}; }
    for (unsigned spin = 0; spin < SPIN_CAP; ++spin) {
      asm volatile("" ::: "memory");
#pragma unroll
      for (int k = 0; k < 4; ++k) if (!ok[k]) gq[k] = __builtin_amdgcn_raw_buffer_load_b128(rws, WS_XQ + ((g * 8 + cbt) * 4 + k) * 512 + pr * 16, 0, 16);
#pragma unroll
      for (int k = 0; k < 4; ++k) ok[k] = tag_ok(gq[k], tag);
      if (__all(ok[0] && ok[1] && ok[2] && ok[3])) break;
      if (QSLEEP) __builtin_amdgcn_s_sleep(QSLEEP);
    }
    { const float q0 = (__uint_as_float(gq[0][1]) + __uint_as_float(gq[1][1])) + (__uint_as_float(gq[2][1]) + __uint_as_float(gq[3][1]));
      const float q1 = (__uint_as_float(gq[0][3]) + __uint_as_float(gq[1][3])) + (__uint_as_float(gq[2][3]) + __uint_as_float(gq[3][3]));
      *(LAS unsigned*)(L + L_Q + ht * 4) = pk2(q0, q1); }
    STAMP(3);
  }
  BARRIER();
  f32x2 wo2[16];
  float xres = 0.f;
  {
    const __amdgpu_buffer_rsrc_t ro = __builtin_amdgcn_make_buffer_rsrc((void*)a.wo, 0, (NOW_ & 2) ? 0 : NQ * D * 2, 0x00020000);
    const unsigned co = ht < 224 ? ht * 4 : 0x7ffffff0u;
    unsigned wov[16];
#pragma unroll
    for (int r = 0; r < 16; ++r) wov[r] = __builtin_amdgcn_raw_buffer_load_b32(ro, co, (unsigned)(g * 512 + s * 16 + r) * (D * 2), WAUX);
    if (b < 56 && ht < 8) xres = bflo((unsigned)a.x[b * 8 + ht]);
    if (hw == 1) {
      const bool has_ffn = s < 28;
      const int j = g * 28 + s, cbh = j >> 3, e = j & 7;
      const __amdgpu_buffer_rsrc_t r1 = __builtin_amdgcn_make_buffer_rsrc((void*)a.w1, 0, has_ffn && !(NOW_ & 4) ? D * DFF * 2 : 0, 0x00020000);
      const __amdgpu_buffer_rsrc_t r2 = __builtin_amdgcn_make_buffer_rsrc((void*)a.w2, 0, has_ffn && !(NOW_ & 8) ? DFF * D * 2 : 0, 0x00020000);
      const int cg = lane & 7, rs = lane >> 3;
      u32x4 w1v[7], w2v[8];
#pragma unroll
      for (int i = 0; i < 7; ++i) w1v[i] = __builtin_amdgcn_raw_buffer_load_b128(r1, (unsigned)(e * 56 + rs + 8 * i) * (DFF * 2) + (unsigned)(cbh * 64 + cg * 8) * 2, 0, WAUX);
#pragma unroll
      for (int r = 0; r < 8; ++r) w2v[r] = __builtin_amdgcn_raw_buffer_load_b128(r2, lane < 56 ? lane * 16 : 0x7ffffff0u, (unsigned)(j * 8 + r) * (D * 2), WAUX);
      float ha[8];
#pragma unroll
      for (int k = 0; k < 8; ++k) ha[k] = 0.f;
#pragma unroll
      for (int i = 0; i < 7; ++i) { const float xr = xn[(has_ffn ? e * 56 : 0) + rs + 8 * i];
#pragma unroll
        for (int k = 0; k < 4; ++k) { ha[2 * k] = fmaf(xr, bflo(w1v[i][k]), ha[2 * k]); ha[2 * k + 1] = fmaf(xr, bfhi(w1v[i][k]), ha[2 * k + 1]); } }
#pragma unroll
      for (int k = 0; k < 4; ++k) ha[k] = sw32_sum(ha[k], ha[k + 4]);
#pragma unroll
      for (int k = 0; k < 2; ++k) ha[k] = sw16_sum(ha[k], ha[k + 2]);
      { const float keep = (lane & 8) ? ha[1] : ha[0], send = (lane & 8) ? ha[0] : ha[1]; ha[0] = keep + dpp<XOR8>(send); }
      const float hn = dpp<XOR8>(ha[0]);
      float hv0 = 0.f, hv1 = 0.f;
      if (has_ffn) {
        if ((lane & 8) == 0)
          __builtin_amdgcn_raw_buffer_store_b128(u32x4{tag, __float_as_uint(ha[0]), tag, __float_as_uint(hn)}, rws,
                                                 WS_XH + j * 512 + (cg * 4 + (lane >> 5) * 2 + ((lane >> 4) & 1)) * 16, 0, 16);
        asm volatile("s_waitcnt vmcnt(0)" ::: "memory");
        u32x4 gh = {0, 0, 0, 0}; bool ok = lane >= 32;
        for (unsigned spin = 0; spin < SPIN_CAP; ++spin) {
          asm volatile("" ::: "memory");
          if (!ok) { gh = __builtin_amdgcn_raw_buffer_load_b128(rws, WS_XH + (cbh * 8 + (lane >> 2)) * 512 + (e * 4 + (lane & 3)) * 16, 0, 16); ok = tag_ok(gh, tag); }
          if (__all(ok)) break;
          __builtin_amdgcn_s_sleep(16);
        }
        hv0 = lane < 32 ? __uint_as_float(gh[1]) : 0.f; hv1 = lane < 32 ? __uint_as_float(gh[3]) : 0.f;
        hv0 += xor4(hv0); hv1 += xor4(hv1); hv0 += dpp<XOR8>(hv0); hv1 += dpp<XOR8>(hv1);
        hv0 = sw16_sum(hv0, hv0); hv1 = sw16_sum(hv1, hv1);
        hv0 = fmaxf(rbf(hv0), 0.f); hv1 = fmaxf(rbf(hv1), 0.f);
      }
      float fo[8];
#pragma unroll
      for (int k = 0; k < 8; ++k) fo[k] = 0.f;
#pragma unroll
      for (int r = 0; r < 8; ++r) { const float hr = rdlane((r & 1) ? hv1 : hv0, r >> 1);
#pragma unroll
        for (int k = 0; k < 4; ++k) { fo[2 * k] = fmaf(hr, bflo(w2v[r][k]), fo[2 * k]); fo[2 * k + 1] = fmaf(hr, bfhi(w2v[r][k]), fo[2 * k + 1]); } }
      if (lane < 56) { LAS float* ff = (LAS float*)(L + L_FFN) + lane * 8;
        *(LAS f32x4*)ff = f32x4{fo[0], fo[1], fo[2], fo[3]}; *(LAS f32x4*)(ff + 4) = f32x4{fo[4], fo[5], fo[6], fo[7]}; }
    }
#pragma unroll
    for (int r = 0; r < 16; ++r) { wo2[r] = f32x2{bflo(wov[r]), bfhi(wov[r])}; asm volatile("" : "+v"(wo2[r])); }
    asm volatile("" : "+v"(xres));
    if (hw == 0) {
      for (unsigned spin = 0; spin < SPIN_CAP * 4; ++spin) { if (__builtin_amdgcn_readfirstlane(lds_ld(cnt + 1)) == 4u) break; __builtin_amdgcn_s_sleep(8); }
      asm volatile("" ::: "memory");
      const int h = lane & 1, ww = (lane >> 1) & 3;
      const f32x2 ml = *(const LAS f32x2*)(L + L_MLW + (ww * 2 + h) * 8);
      float M = fmaxf(ml[0], dpp<XOR2>(ml[0])); M = fmaxf(M, xor4(M));
      float Ls = __expf(ml[0] - M) * ml[1]; Ls += dpp<XOR2>(Ls); Ls += xor4(Ls);
      if (lane < 2) { const u32x4 gr = {tag, __float_as_uint(M), tag, __float_as_uint(Ls)};
        __builtin_amdgcn_raw_buffer_store_b128(gr, rws, WS_ML + (g * 32 + s) * 32 + lane * 16, 0, 16); }
      asm volatile("s_waitcnt vmcnt(0)" ::: "memory");
      u32x4 gm = {0, 0, 0, 0}; bool ok = false;
      for (unsigned spin = 0; spin < SPIN_CAP * 4; ++spin) {
        asm volatile("" ::: "memory");
        if (!ok) gm = __builtin_amdgcn_raw_buffer_load_b128(rws, WS_ML + g * 1024 + lane * 16, 0, 16);
        ok = tag_ok(gm, tag);
        if (__all(ok)) break;
        __builtin_amdgcn_s_sleep(MLSLEEP);
      }
      const float ms = __uint_as_float(gm[1]);
      float Mg = fmaxf(ms, dpp<XOR2>(ms)); Mg = fmaxf(Mg, xor4(Mg)); Mg = fmaxf(Mg, dpp<XOR8>(Mg)); Mg = sw16_max(Mg); Mg = sw32_max(Mg);
      float Lg = __expf(ms - Mg) * __uint_as_float(gm[3]);
      Lg += dpp<XOR2>(Lg); Lg += xor4(Lg); Lg += dpp<XOR8>(Lg); Lg = sw16_sum(Lg, Lg); Lg = sw32_sum(Lg, Lg);
      if (lane < 8) ((LAS float*)(L + L_FAC))[h * 4 + ww] = __expf(ml[0] - Mg) / Lg;
    }
  }
  BARRIER();
  STAMP(6);
  f32x2 o2;
  {
    const int h = ht >> 7, p = ht & 127;
    const f32x4 fac = *(const LAS f32x4*)(L + L_FAC + h * 16);
    const LAS float* ar = (const LAS float*)(L + L_ACC) + h * 256 + 2 * p;
    f32x2 O = f32x2{fac[0], fac[0]} * *(const LAS f32x2*)ar;
#pragma unroll
    for (int k = 1; k < 4; ++k) O = __builtin_elementwise_fma(f32x2{fac[k], fac[k]}, *(const LAS f32x2*)(ar + k * 512), O);
    o2 = *(const LAS f32x2*)(L + L_FFN + (ht < 224 ? ht : 0) * 8);
    const u32x4 gr = {tag, __float_as_uint(O[0]), tag, __float_as_uint(O[1])};
    __builtin_amdgcn_raw_buffer_store_b128(gr, rws, WS_X1 + (g * 32 + s) * 4096 + ht * 16, 0, SAUX);
  }
  if (DRAIN1) asm volatile("s_waitcnt vmcnt(0)" ::: "memory");
  if (DELAY1) __builtin_amdgcn_s_sleep(DELAY1);
  STAMP(7);
  {
    const int pp = lane & 7, sq = lane >> 3;
    const unsigned ao = WS_X1 + (g * 32 + sq) * 4096 + s * 128 + pp * 16;
    u32x4 go[4]; bool ok[4] = {false, false, false, false};
#pragma unroll
    for (int k = 0; k < 4; ++k) go[k] = u32x4{0, 0, 0, 0};
    for (unsigned spin = 0; spin < SPIN_CAP; ++spin) {
      asm volatile("" ::: "memory");
#pragma unroll
      for (int k = 0; k < 4; ++k) if (!ok[k]) go[k] = __builtin_amdgcn_raw_buffer_load_b128(rws, ao + k * 8 * 4096, 0, 16);
#pragma unroll
      for (int k = 0; k < 4; ++k) ok[k] = tag_ok(go[k], tag);
      if (__all(ok[0] && ok[1] && ok[2] && ok[3])) break;
      if (SLEEP1) __builtin_amdgcn_s_sleep(SLEEP1);
    }
    STAMP(8);
    float a0 = (__uint_as_float(go[0][1]) + __uint_as_float(go[1][1])) + (__uint_as_float(go[2][1]) + __uint_as_float(go[3][1]));
    float a1 = (__uint_as_float(go[0][3]) + __uint_as_float(go[1][3])) + (__uint_as_float(go[2][3]) + __uint_as_float(go[3][3]));
    a0 += dpp<XOR8>(a0); a1 += dpp<XOR8>(a1);
    { const float t = sw16_sum(a0, a1);
      const float t2 = rbf(sw32_sum(t, t));
#pragma unroll
      for (int r = 0; r < 16; ++r) { const float orr = rdlane(t2, (r & 1) * 16 + (r >> 1)); o2 = __builtin_elementwise_fma(f32x2{orr, orr}, wo2[r], o2); } }
    const u32x4 gr = {tag, __float_as_uint(o2[0]), tag, __float_as_uint(o2[1])};
    __builtin_amdgcn_raw_buffer_store_b128(gr, rws, ht < 224 ? WS_X2 + b * 3584 + ht * 16 : 0x7ffffff0u, 0, SAUX);
  }
  if (DRAIN2) asm volatile("s_waitcnt vmcnt(0)" ::: "memory");
  if (DELAY2) __builtin_amdgcn_s_sleep(DELAY2);
  STAMP(9);
  if (b < 56) {
    const int pos = ht & 3, sr = ht >> 2;
    u32x4 gv[4]; bool ok[4] = {false, false, false, false};
#pragma unroll
    for (int k = 0; k < 4; ++k) gv[k] = u32x4{0, 0, 0, 0};
    for (unsigned spin = 0; spin < SPIN_CAP; ++spin) {
      asm volatile("" ::: "memory");
#pragma unroll
      for (int k = 0; k < 4; ++k) if (!ok[k]) gv[k] = __builtin_amdgcn_raw_buffer_load_b128(rws, WS_X2 + (sr + 64 * k) * 3584 + b * 64 + pos * 16, 0, 16);
#pragma unroll
      for (int k = 0; k < 4; ++k) ok[k] = tag_ok(gv[k], tag);
      if (__all(ok[0] && ok[1] && ok[2] && ok[3])) break;
    }
    STAMP(10);
    float a0 = (__uint_as_float(gv[0][1]) + __uint_as_float(gv[1][1])) + (__uint_as_float(gv[2][1]) + __uint_as_float(gv[3][1]));
    float a1 = (__uint_as_float(gv[0][3]) + __uint_as_float(gv[1][3])) + (__uint_as_float(gv[2][3]) + __uint_as_float(gv[3][3]));
    a0 += xor4(a0); a1 += xor4(a1); a0 += dpp<XOR8>(a0); a1 += dpp<XOR8>(a1);
    { const float t = sw16_sum(a0, a1); const float t2 = sw32_sum(t, t);
      LAS float* fin = (LAS float*)(L + L_FIN);
      if ((lane & 0x2c) == 0) fin[hw * 8 + 2 * (lane & 3) + (lane >> 4)] = t2;
      BARRIER();
      if (ht < 8) {
        const float r = xres + ((fin[ht] + fin[8 + ht]) + (fin[16 + ht] + fin[24 + ht]));
        ((GAS unsigned short*)a.out)[b * 8 + ht] = __builtin_bit_cast(unsigned short, (__bf16)r);
      } }
    if (b == 0 && ht == 0) __builtin_amdgcn_raw_buffer_store_b32(tag, rws, WS_EPOCH, 0, 16);
  }
}

extern "C" void kernel_launch(void* const* d_in, const int* in_sizes, int n_in, void* d_out, int out_size, void* d_ws, size_t ws_size, hipStream_t stream) {
  Args a;
  a.x = (const unsigned short*)d_in[0]; a.kc = (const unsigned short*)d_in[1]; a.vc = (const unsigned short*)d_in[2];
  a.wq = (const unsigned short*)d_in[3]; a.wo = (const unsigned short*)d_in[4]; a.w1 = (const unsigned short*)d_in[6]; a.w2 = (const unsigned short*)d_in[7];
  a.out = (unsigned short*)d_out; a.ws = (unsigned char*)d_ws;
  (void)hipFuncSetAttribute((const void*)fused_decode, hipFuncAttributeMaxDynamicSharedMemorySize, LDS_BYTES);
  hipLaunchKernelGGL(fused_decode, dim3(NWG), dim3(NTHR), LDS_BYTES, stream, a);
}
```

```cpp
#define TFIX 6000
#define LDSKB 28
#include <hip/hip_runtime.h>
#include <stdint.h>

typedef unsigned u32x4 __attribute__((ext_vector_type(4)));
typedef unsigned u32x2 __attribute__((ext_vector_type(2)));
typedef float f32x4 __attribute__((ext_vector_type(4)));
typedef float f32x2 __attribute__((ext_vector_type(2)));
typedef __bf16 bf16x2 __attribute__((ext_vector_type(2)));
#define LAS __attribute__((address_space(3)))
#define GAS __attribute__((address_space(1)))

#define P0 6
#define ROT 1
#define WAUX 2
#define STAMPS 0
#define NOQ 0
#define NOCK 0
#define NOCV 0
#define NOW_ 0
#define SW 0
#define DRAIN 0
#define QSLEEP 2
#define MLSLEEP 16
#define NOML 0
#define AUXQ WAUX
#define AUX1 WAUX
#define TAU 3790
#define TOFF 280
#define PG 4
#define DRAIN1 1
#define DRAIN2 1
#define DELAY1 0
#define DELAY2 0
#define SAUX 16
#define SLEEP1 1

constexpr int D = 448, SK = 32768, DH = 256, DFF = 1792, NQ = 4096;
#define WIN 16
constexpr int NWG = 256, NTHR = 512, W = WIN, NIT = 128 / W, NG = W / 16;
constexpr float EPS = 1e-5f;
constexpr unsigned SPIN_CAP = 1u << 17;
constexpr unsigned WS_EPOCH = 0, WS_ML = 4096, WS_XQ = 16384, WS_XH = WS_XQ + 131072, WS_X1 = 524288, WS_X2 = WS_X1 + (1u << 20), WS_STAMP = 3u << 20, WS_USED = 4u << 20;
static_assert(WS_ML + 8192 <= WS_XQ && WS_XH + 224 * 512 <= WS_X1 && WS_X2 + 256 * 3584 <= WS_STAMP, "ws");
constexpr unsigned L_Q = 0;
constexpr unsigned L_XN = 1024;
constexpr unsigned L_QP = L_XN + 4 * 448 * 4;
constexpr unsigned L_CNT = L_QP + 1024;
constexpr unsigned L_MLW = L_CNT + 16;
constexpr unsigned L_FAC = L_MLW + 64;
constexpr unsigned L_FFN = L_FAC + 32;
constexpr unsigned L_FIN = L_FFN + 448 * 4;
constexpr unsigned L_P = L_FIN + 128;
constexpr unsigned L_ACC = L_P + 8192;
constexpr unsigned L_END = L_ACC + 8192;
constexpr unsigned LDS_BYTES = LDSKB * 1024;
static_assert(L_END <= LDS_BYTES && L_P % 16 == 0 && L_FFN % 16 == 0 && L_FAC % 16 == 0 && L_MLW % 16 == 0, "lds");

__device__ __forceinline__ float bflo(unsigned u) { return __builtin_bit_cast(float, u << 16); }
__device__ __forceinline__ float bfhi(unsigned u) { return __builtin_bit_cast(float, u & 0xffff0000u); }
__device__ __forceinline__ float rbf(float f) { return (float)(__bf16)f; }
__device__ __forceinline__ unsigned pk2(float lo, float hi) { bf16x2 v = {(__bf16)lo, (__bf16)hi}; return __builtin_bit_cast(unsigned, v); }
__device__ __forceinline__ float dot2(unsigned a, unsigned b, float c) {
  return __builtin_amdgcn_fdot2_f32_bf16(__builtin_bit_cast(bf16x2, a), __builtin_bit_cast(bf16x2, b), c, false);
}
template <int CTRL> __device__ __forceinline__ float dpp(float x) {
  return __builtin_bit_cast(float, __builtin_amdgcn_mov_dpp(__builtin_bit_cast(int, x), CTRL, 0xf, 0xf, true));
}
constexpr int XOR1 = 0xB1, XOR2 = 0x4E, XOR3 = 0x1B, XOR7 = 0x141, XOR8 = 0x128, ROR4 = 0x124;
__device__ __forceinline__ float xor4(float x) { return dpp<XOR3>(dpp<XOR7>(x)); }
__device__ __forceinline__ float sw16_sum(float a, float b) {
  auto r = __builtin_amdgcn_permlane16_swap(__float_as_uint(a), __float_as_uint(b), false, false);
  return __uint_as_float(r[0]) + __uint_as_float(r[1]);
}
__device__ __forceinline__ float sw32_sum(float a, float b) {
  auto r = __builtin_amdgcn_permlane32_swap(__float_as_uint(a), __float_as_uint(b), false, false);
  return __uint_as_float(r[0]) + __uint_as_float(r[1]);
}
__device__ __forceinline__ float sw16_max(float a) {
  auto r = __builtin_amdgcn_permlane16_swap(__float_as_uint(a), __float_as_uint(a), false, false);
  return fmaxf(__uint_as_float(r[0]), __uint_as_float(r[1]));
}
__device__ __forceinline__ float sw32_max(float a) {
  auto r = __builtin_amdgcn_permlane32_swap(__float_as_uint(a), __float_as_uint(a), false, false);
  return fmaxf(__uint_as_float(r[0]), __uint_as_float(r[1]));
}
__device__ __forceinline__ float rdlane(float v, int l) { return __builtin_bit_cast(float, __builtin_amdgcn_readlane(__builtin_bit_cast(int, v), l)); }
#define BARRIER() do { __builtin_amdgcn_sched_barrier(0); asm volatile("s_waitcnt lgkmcnt(0)\n\ts_barrier" ::: "memory"); __builtin_amdgcn_sched_barrier(0); } while (0)
#define NOW() __builtin_amdgcn_s_memrealtime()
__device__ __forceinline__ void pace(unsigned tgt) {
  asm volatile("pace_%=:\n\ts_memrealtime s[60:61]\n\ts_waitcnt lgkmcnt(0)\n\ts_sub_u32 s60, s60, %0\n\ts_cmp_lt_i32 s60, 0\n\ts_cbranch_scc1 pace_%=" :: "s"(tgt) : "memory", "scc", "s60", "s61");
}
__device__ __forceinline__ unsigned lds_ld(LAS unsigned* p) { return __hip_atomic_load(p, __ATOMIC_RELAXED, __HIP_MEMORY_SCOPE_WORKGROUP); }
__device__ __forceinline__ void lds_inc(LAS unsigned* p) { (void)__hip_atomic_fetch_add(p, 1u, __ATOMIC_RELAXED, __HIP_MEMORY_SCOPE_WORKGROUP); }
__device__ __forceinline__ bool tag_ok(const u32x4& v, unsigned tag) { return v[0] == tag && v[2] == tag; }

struct Args {
  const unsigned short *x, *kc, *vc, *wq, *wo, *w1, *w2;
  unsigned short* out;
  unsigned char* ws;
};

__global__ void __launch_bounds__(NTHR, 2) fused_decode(Args a) {
  extern __shared__ __attribute__((aligned(16))) unsigned char smem[];
  LAS unsigned char* L = (LAS unsigned char*)smem;
  const int tid = threadIdx.x, lane = tid & 63, w = __builtin_amdgcn_readfirstlane(tid >> 6);
  const int b = blockIdx.x, g = b & 7, s = b >> 3;
#define STAMP(i)

  if (w < 4) {
    const int hf = lane >> 5, c = lane & 31;
    const unsigned voff = lane * 16;
    const unsigned sbase = (unsigned)s * (1024u * 512u) + (unsigned)w * 2048u;
    const int rp = (s * ROT) & 63;
    const unsigned short* kh = a.kc + (size_t)g * SK * DH;
    const unsigned short* vh = a.vc + (size_t)g * SK * DH;
    constexpr int HEADB = SK * DH * 2;
    auto soff = [&](int p) -> unsigned { return sbase + ((unsigned)((p + rp) & 63) << 13); };
    u32x4 buf[W];
    const unsigned tb = (unsigned)NOW() + TOFF;
    BARRIER();
    {
      const __amdgpu_buffer_rsrc_t rk = __builtin_amdgcn_make_buffer_rsrc((void*)kh, 0, HEADB, 0x00020000);
#pragma unroll
      for (int i = 0; i < W; ++i) {
        if (i >= P0) asm volatile("s_waitcnt vmcnt(%0)" ::"i"(P0 - 1) : "memory");
        buf[i] = __builtin_amdgcn_raw_buffer_load_b128(rk, voff + (i & 1) * 1024, soff(i >> 1), 2);
        __builtin_amdgcn_sched_barrier(0);
      }
    }
    BARRIER();
    const u32x4 q0 = *(const LAS u32x4*)(L + L_Q + c * 16), q1 = *(const LAS u32x4*)(L + L_Q + 512 + c * 16);
    LAS float* pl = (LAS float*)(L + L_P) + w * 512 + hf * 256;
    float m = -INFINITY;
#pragma unroll 1
    for (int it = 0; it < NIT; ++it) {
      const bool tok = it < NIT - 1;
      const __amdgpu_buffer_rsrc_t rn = __builtin_amdgcn_make_buffer_rsrc((void*)(tok ? kh : vh), 0, HEADB, 0x00020000);
      const int pb = tok ? (it + 1) * (W / 2) : 0;
#pragma unroll
      for (int gr = 0; gr < NG; ++gr) {
        float x[32];
#pragma unroll
        for (int i = 0; i < 16; ++i) {
          const int idx = gr * 16 + i;
          float s0 = 0.f, s1 = 0.f;
#pragma unroll
          for (int k = 0; k < 4; ++k) { s0 = dot2(buf[idx][k], q0[k], s0); s1 = dot2(buf[idx][k], q1[k], s1); }
          x[2 * i] = s0; x[2 * i + 1] = s1;
          if (TAU && (idx % PG) == 0) { asm volatile("" : "+v"(x[2 * i]), "+v"(x[2 * i + 1]) :: "memory"); pace(tb + (((unsigned)(it * W + idx) * TAU) >> 8)); }
          buf[idx] = __builtin_amdgcn_raw_buffer_load_b128(rn, voff + (idx & 1) * 1024, soff(pb + (idx >> 1)), 2);
          __builtin_amdgcn_sched_barrier(0);
        }
#pragma unroll
        for (int hh = 0; hh < 16; ++hh) x[hh] = sw16_sum(x[hh], x[hh + 16]);
#define TR(HALF, CTRL, BIT) _Pragma("unroll") for (int hh = 0; hh < (HALF); ++hh) { \
          const float keep = (c & (BIT)) ? x[hh + (HALF)] : x[hh], send = (c & (BIT)) ? x[hh] : x[hh + (HALF)]; x[hh] = keep + dpp<CTRL>(send); }
        TR(8, XOR8, 8) TR(4, XOR7, 4) TR(2, XOR2, 2) TR(1, XOR1, 1)
#undef TR
        const float sv = rbf(x[0]) * 0.0625f;
        m = fmaxf(m, sv);
        pl[(it * NG + gr) * 32 + c] = sv;
        __builtin_amdgcn_sched_barrier(0);
      }
    }
    STAMP(4);
    m = fmaxf(m, dpp<XOR2>(m)); m = fmaxf(m, dpp<ROR4>(m)); m = fmaxf(m, dpp<XOR8>(m)); m = sw16_max(m); m = sw32_max(m);
    float l = 0.f;
    {
      float sc[8];
#pragma unroll
      for (int j = 0; j < 8; ++j) sc[j] = pl[j * 32 + c];
#pragma unroll
      for (int j = 0; j < 8; ++j) { const float p = __expf(sc[j] - m); l += p; pl[j * 32 + c] = p; }
    }
    l += dpp<XOR2>(l); l += dpp<ROR4>(l); l += dpp<XOR8>(l); l = sw16_sum(l, l); l = sw32_sum(l, l);
    if (lane < 2) *(LAS f32x2*)(L + L_MLW + (w * 2 + lane) * 8) = f32x2{m, l};
    if (lane == 0) lds_inc((LAS unsigned*)(L + L_CNT) + 1);
    asm volatile("s_waitcnt lgkmcnt(0)" ::: "memory");
    f32x2 ac2[8];
#pragma unroll
    for (int k = 0; k < 8; ++k) ac2[k] = f32x2{0.f, 0.f};
#pragma unroll 1
    for (int it = 0; it < NIT; ++it) {
      const __amdgpu_buffer_rsrc_t rn = __builtin_amdgcn_make_buffer_rsrc((void*)vh, 0, it < NIT - 1 ? HEADB : 0, 0x00020000);
      const int pb = (it + 1) * (W / 2);
      const unsigned tv = tb + ((((unsigned)(128 + it * W) * TAU) >> 8) & (it < NIT - 1 ? ~0u : 0u));
      f32x4 pp = *(const LAS f32x4*)(pl + it * W * 2);
#pragma unroll
      for (int i = 0; i < W; i += 2) {
        const f32x4 pc = pp;
        if (i + 2 < W) pp = *(const LAS f32x4*)(pl + (it * W + i + 2) * 2);
#pragma unroll
        for (int u = 0; u < 2; ++u) {
          const f32x2 pa = {pc[2 * u], pc[2 * u]}, pb2 = {pc[2 * u + 1], pc[2 * u + 1]};
#pragma unroll
          for (int k = 0; k < (NOCV ? 1 : 4); ++k) {
            const f32x2 vv = NOCV ? f32x2{__uint_as_float(buf[i + u][0] ^ buf[i + u][1]), __uint_as_float(buf[i + u][2] ^ buf[i + u][3])} : f32x2{bflo(buf[i + u][k]), bfhi(buf[i + u][k])};
            ac2[k] = __builtin_elementwise_fma(pa, vv, ac2[k]);
            ac2[4 + k] = __builtin_elementwise_fma(pb2, vv, ac2[4 + k]);
          }
          asm volatile("" : "+v"(ac2[0]), "+v"(ac2[1]), "+v"(ac2[2]), "+v"(ac2[3]), "+v"(ac2[4]), "+v"(ac2[5]), "+v"(ac2[6]), "+v"(ac2[7]) :: "memory");
          if (TAU && ((i + u) % PG) == 0) pace(tv + (((unsigned)(i + u) * TAU) >> 8));
          buf[i + u] = __builtin_amdgcn_raw_buffer_load_b128(rn, voff + u * 1024, soff(pb + (i >> 1)), 2);
          __builtin_amdgcn_sched_barrier(0);
        }
      }
    }
    STAMP(5);
    {
      float acc[8];
#pragma unroll
      for (int k = 0; k < 4; ++k) { acc[2 * k] = sw32_sum(ac2[k][0], ac2[4 + k][0]); acc[2 * k + 1] = sw32_sum(ac2[k][1], ac2[4 + k][1]); }
      LAS float* rec = (LAS float*)(L + L_ACC) + (w * 2 + hf) * 256 + 8 * c;
      *(LAS f32x4*)rec = f32x4{acc[0], acc[1], acc[2], acc[3]};
      *(LAS f32x4*)(rec + 4) = f32x4{acc[4], acc[5], acc[6], acc[7]};
    }
    BARRIER();
    return;
  }

  STAMP(0);
  const unsigned tfix0 = (unsigned)NOW();
  const int hw = w - 4, ht = tid - 256;
  const __amdgpu_buffer_rsrc_t rws = __builtin_amdgcn_make_buffer_rsrc((void*)a.ws, 0, WS_USED, 0x00020000);
  LAS unsigned* cnt = (LAS unsigned*)(L + L_CNT);
  LAS float* xn = (LAS float*)(L + L_XN) + hw * 448;
  unsigned tag;
  {
    const int cb = s >> 2, kq = s & 3;
    const __amdgpu_buffer_rsrc_t rx = __builtin_amdgcn_make_buffer_rsrc((void*)a.x, 0, D * 2, 0x00020000);
    const __amdgpu_buffer_rsrc_t rq = __builtin_amdgcn_make_buffer_rsrc((void*)(a.wq + (size_t)kq * 112 * NQ), 0, (NOW_ & 1) ? 0 : 112 * NQ * 2, 0x00020000);
    const u32x4 xv = __builtin_amdgcn_raw_buffer_load_b128(rx, lane * 16, 0, 0);
    const int cg = ht & 7, rs = ht >> 3;
    u32x4 wv[4];
    const unsigned qo = (unsigned)rs * (NQ * 2) + (unsigned)(g * 512 + cb * 64 + cg * 8) * 2;
#pragma unroll
    for (int i = 0; i < 4; ++i) wv[i] = __builtin_amdgcn_raw_buffer_load_b128(rq, qo + i * 32 * (NQ * 2), 0, WAUX);
    const unsigned ep = __builtin_amdgcn_raw_buffer_load_b32(rws, 0, WS_EPOCH, 16);
    if (ht < 2) cnt[ht] = 0u;
    BARRIER();
    STAMP(12);
    float f[8];
#pragma unroll
    for (int i = 0; i < 4; ++i) { f[2 * i] = bflo(xv[i]); f[2 * i + 1] = bfhi(xv[i]); }
    const float x0 = rdlane(f[0], 0);
    float s1 = 0.f, s2 = 0.f;
#pragma unroll
    for (int i = 0; i < 8; ++i) { const float d = f[i] - x0; s1 += d; s2 = fmaf(d, d, s2); }
    if (lane >= 56) { s1 = 0.f; s2 = 0.f; }
    s1 += dpp<XOR1>(s1); s2 += dpp<XOR1>(s2); s1 += dpp<XOR2>(s1); s2 += dpp<XOR2>(s2);
    s1 += dpp<XOR7>(s1); s2 += dpp<XOR7>(s2); s1 += dpp<XOR8>(s1); s2 += dpp<XOR8>(s2);
    { const float t = sw16_sum(s1, s2);
      const float t2 = sw32_sum(t, t); s1 = rdlane(t2, 0); s2 = rdlane(t2, 16); }
    const float md = s1 * (1.0f / D);
    const float var = fmaxf(fmaf(-md, md, s2 * (1.0f / D)), 0.f);
    const float mb = rbf(x0 + md), den = rbf(sqrtf(rbf(rbf(var) + EPS)));
    const float rden = 1.0f / den;
    if (lane < 56) {
      f32x4 n0, n1;
#pragma unroll
      for (int i = 0; i < 4; ++i) { n0[i] = rbf(rbf(f[i] - mb) * rden); n1[i] = rbf(rbf(f[4 + i] - mb) * rden); }
      *(LAS f32x4*)(xn + lane * 8) = n0; *(LAS f32x4*)(xn + lane * 8 + 4) = n1;
    }
    asm volatile("" ::: "memory");
    STAMP(14);
    float xr[4];
#pragma unroll
    for (int i = 0; i < 4; ++i) { const int row = rs + 32 * i; xr[i] = xn[kq * 112 + (row < 112 ? row : 0)]; }
    float qa[8];
#pragma unroll
    for (int k = 0; k < 8; ++k) qa[k] = 0.f;
#pragma unroll
    for (int i = 0; i < 4; ++i) {
#pragma unroll
      for (int k = 0; k < 4; ++k) { qa[2 * k] = fmaf(xr[i], bflo(wv[i][k]), qa[2 * k]); qa[2 * k + 1] = fmaf(xr[i], bfhi(wv[i][k]), qa[2 * k + 1]); }
    }
#pragma unroll
    for (int k = 0; k < 4; ++k) qa[k] = sw32_sum(qa[k], qa[k + 4]);
#pragma unroll
    for (int k = 0; k < 2; ++k) qa[k] = sw16_sum(qa[k], qa[k + 2]);
    { const float keep = (lane & 8) ? qa[1] : qa[0], send = (lane & 8) ? qa[0] : qa[1]; qa[0] = keep + dpp<XOR8>(send); }
    LAS float* qp = (LAS float*)(L + L_QP);
    qp[hw * 64 + cg * 8 + (lane >> 5) * 4 + ((lane >> 4) & 1) * 2 + ((lane >> 3) & 1)] = qa[0];
    if (lane == 0) lds_inc(cnt);
    STAMP(1);
    tag = __builtin_amdgcn_readfirstlane(ep) + 1u;
    for (unsigned spin = 0; spin < SPIN_CAP; ++spin) { if (__builtin_amdgcn_readfirstlane(lds_ld(cnt)) == 4u) break; }
    asm volatile("" ::: "memory");
    const int pr = ht & 31, cbt = ht >> 5;
    f32x2 own = *(const LAS f32x2*)(qp + 2 * pr);
#pragma unroll
    for (int k = 1; k < 4; ++k) own += *(const LAS f32x2*)(qp + k * 64 + 2 * pr);
    if (cbt == 2 * hw && (lane >> 3) == hw)
      __builtin_amdgcn_raw_buffer_store_b128(u32x4{tag, __float_as_uint(own[0]), tag, __float_as_uint(own[1])}, rws, WS_XQ + ((g * 8 + cb) * 4 + kq) * 512 + pr * 16, 0, 16);
    STAMP(2);
    u32x4 gq[4]; bool ok[4];
#pragma unroll
    for (int k = 0; k < 4; ++k) { ok[k] = cbt == cb && k == kq; gq[k] = u32x4{tag, __float_as_uint(own[0]), tag, __float_as_uint(own[1])}; }
    for (unsigned spin = 0; spin < SPIN_CAP; ++spin) {
      asm volatile("" ::: "memory");
#pragma unroll
      for (int k = 0; k < 4; ++k) if (!ok[k]) gq[k] = __builtin_amdgcn_raw_buffer_load_b128(rws, WS_XQ + ((g * 8 + cbt) * 4 + k) * 512 + pr * 16, 0, 16);
#pragma unroll
      for (int k = 0; k < 4; ++k) ok[k] = tag_ok(gq[k], tag);
      if (__all(ok[0] && ok[1] && ok[2] && ok[3])) break;
      if (QSLEEP) __builtin_amdgcn_s_sleep(QSLEEP);
    }
    { const float q0 = (__uint_as_float(gq[0][1]) + __uint_as_float(gq[1][1])) + (__uint_as_float(gq[2][1]) + __uint_as_float(gq[3][1]));
      const float q1 = (__uint_as_float(gq[0][3]) + __uint_as_float(gq[1][3])) + (__uint_as_float(gq[2][3]) + __uint_as_float(gq[3][3]));
      *(LAS unsigned*)(L + L_Q + ht * 4) = pk2(q0, q1); }
    STAMP(3);
  }
  BARRIER();
  f32x2 wo2[16];
  float xres = 0.f;
  {
    const __amdgpu_buffer_rsrc_t ro = __builtin_amdgcn_make_buffer_rsrc((void*)a.wo, 0, (NOW_ & 2) ? 0 : NQ * D * 2, 0x00020000);
    const unsigned co = ht < 224 ? ht * 4 : 0x7ffffff0u;
    unsigned wov[16];
#pragma unroll
    for (int r = 0; r < 16; ++r) wov[r] = __builtin_amdgcn_raw_buffer_load_b32(ro, co, (unsigned)(g * 512 + s * 16 + r) * (D * 2), WAUX);
    if (b < 56 && ht < 8) xres = bflo((unsigned)a.x[b * 8 + ht]);
    if (hw == 1) {
      const bool has_ffn = s < 28;
      const int j = g * 28 + s, cbh = j >> 3, e = j & 7;
      const __amdgpu_buffer_rsrc_t r1 = __builtin_amdgcn_make_buffer_rsrc((void*)a.w1, 0, has_ffn && !(NOW_ & 4) ? D * DFF * 2 : 0, 0x00020000);
      const __amdgpu_buffer_rsrc_t r2 = __builtin_amdgcn_make_buffer_rsrc((void*)a.w2, 0, has_ffn && !(NOW_ & 8) ? DFF * D * 2 : 0, 0x00020000);
      const int cg = lane & 7, rs = lane >> 3;
      u32x4 w1v[7], w2v[8];
#pragma unroll
      for (int i = 0; i < 7; ++i) w1v[i] = __builtin_amdgcn_raw_buffer_load_b128(r1, (unsigned)(e * 56 + rs + 8 * i) * (DFF * 2) + (unsigned)(cbh * 64 + cg * 8) * 2, 0, WAUX);
#pragma unroll
      for (int r = 0; r < 8; ++r) w2v[r] = __builtin_amdgcn_raw_buffer_load_b128(r2, lane < 56 ? lane * 16 : 0x7ffffff0u, (unsigned)(j * 8 + r) * (D * 2), WAUX);
      float ha[8];
#pragma unroll
      for (int k = 0; k < 8; ++k) ha[k] = 0.f;
#pragma unroll
      for (int i = 0; i < 7; ++i) { const float xr = xn[(has_ffn ? e * 56 : 0) + rs + 8 * i];
#pragma unroll
        for (int k = 0; k < 4; ++k) { ha[2 * k] = fmaf(xr, bflo(w1v[i][k]), ha[2 * k]); ha[2 * k + 1] = fmaf(xr, bfhi(w1v[i][k]), ha[2 * k + 1]); } }
#pragma unroll
      for (int k = 0; k < 4; ++k) ha[k] = sw32_sum(ha[k], ha[k + 4]);
#pragma unroll
      for (int k = 0; k < 2; ++k) ha[k] = sw16_sum(ha[k], ha[k + 2]);
      { const float keep = (lane & 8) ? ha[1] : ha[0], send = (lane & 8) ? ha[0] : ha[1]; ha[0] = keep + dpp<XOR8>(send); }
      const float hn = dpp<XOR8>(ha[0]);
      float hv0 = 0.f, hv1 = 0.f;
      if (has_ffn) {
        if ((lane & 8) == 0)
          __builtin_amdgcn_raw_buffer_store_b128(u32x4{tag, __float_as_uint(ha[0]), tag, __float_as_uint(hn)}, rws,
                                                 WS_XH + j * 512 + (cg * 4 + (lane >> 5) * 2 + ((lane >> 4) & 1)) * 16, 0, 16);
        asm volatile("s_waitcnt vmcnt(0)" ::: "memory");
        u32x4 gh = {0, 0, 0, 0}; bool ok = lane >= 32;
        for (unsigned spin = 0; spin < SPIN_CAP; ++spin) {
          asm volatile("" ::: "memory");
          if (!ok) { gh = __builtin_amdgcn_raw_buffer_load_b128(rws, WS_XH + (cbh * 8 + (lane >> 2)) * 512 + (e * 4 + (lane & 3)) * 16, 0, 16); ok = tag_ok(gh, tag); }
          if (__all(ok)) break;
          __builtin_amdgcn_s_sleep(16);
        }
        hv0 = lane < 32 ? __uint_as_float(gh[1]) : 0.f; hv1 = lane < 32 ? __uint_as_float(gh[3]) : 0.f;
        hv0 += xor4(hv0); hv1 += xor4(hv1); hv0 += dpp<XOR8>(hv0); hv1 += dpp<XOR8>(hv1);
        hv0 = sw16_sum(hv0, hv0); hv1 = sw16_sum(hv1, hv1);
        hv0 = fmaxf(rbf(hv0), 0.f); hv1 = fmaxf(rbf(hv1), 0.f);
      }
      float fo[8];
#pragma unroll
      for (int k = 0; k < 8; ++k) fo[k] = 0.f;
#pragma unroll
      for (int r = 0; r < 8; ++r) { const float hr = rdlane((r & 1) ? hv1 : hv0, r >> 1);
#pragma unroll
        for (int k = 0; k < 4; ++k) { fo[2 * k] = fmaf(hr, bflo(w2v[r][k]), fo[2 * k]); fo[2 * k + 1] = fmaf(hr, bfhi(w2v[r][k]), fo[2 * k + 1]); } }
      if (lane < 56) { LAS float* ff = (LAS float*)(L + L_FFN) + lane * 8;
        *(LAS f32x4*)ff = f32x4{fo[0], fo[1], fo[2], fo[3]}; *(LAS f32x4*)(ff + 4) = f32x4{fo[4], fo[5], fo[6], fo[7]}; }
    }
#pragma unroll
    for (int r = 0; r < 16; ++r) { wo2[r] = f32x2{bflo(wov[r]), bfhi(wov[r])}; asm volatile("" : "+v"(wo2[r])); }
    asm volatile("" : "+v"(xres));
    if (hw == 0) {
      for (unsigned spin = 0; spin < SPIN_CAP * 4; ++spin) { if (__builtin_amdgcn_readfirstlane(lds_ld(cnt + 1)) == 4u) break; __builtin_amdgcn_s_sleep(8); }
      asm volatile("" ::: "memory");
      const int h = lane & 1, ww = (lane >> 1) & 3;
      const f32x2 ml = *(const LAS f32x2*)(L + L_MLW + (ww * 2 + h) * 8);
      float M = fmaxf(ml[0], dpp<XOR2>(ml[0])); M = fmaxf(M, xor4(M));
      float Ls = __expf(ml[0] - M) * ml[1]; Ls += dpp<XOR2>(Ls); Ls += xor4(Ls);
      if (lane < 2) { const u32x4 gr = {tag, __float_as_uint(M), tag, __float_as_uint(Ls)};
        __builtin_amdgcn_raw_buffer_store_b128(gr, rws, WS_ML + (g * 32 + s) * 32 + lane * 16, 0, 16); }
      asm volatile("s_waitcnt vmcnt(0)" ::: "memory");
      u32x4 gm = {0, 0, 0, 0}; bool ok = false;
      for (unsigned spin = 0; spin < SPIN_CAP * 4; ++spin) {
        asm volatile("" ::: "memory");
        if (!ok) gm = __builtin_amdgcn_raw_buffer_load_b128(rws, WS_ML + g * 1024 + lane * 16, 0, 16);
        ok = tag_ok(gm, tag);
        if (__all(ok)) break;
        __builtin_amdgcn_s_sleep(MLSLEEP);
      }
      const float ms = __uint_as_float(gm[1]);
      float Mg = fmaxf(ms, dpp<XOR2>(ms)); Mg = fmaxf(Mg, xor4(Mg)); Mg = fmaxf(Mg, dpp<XOR8>(Mg)); Mg = sw16_max(Mg); Mg = sw32_max(Mg);
      float Lg = __expf(ms - Mg) * __uint_as_float(gm[3]);
      Lg += dpp<XOR2>(Lg); Lg += xor4(Lg); Lg += dpp<XOR8>(Lg); Lg = sw16_sum(Lg, Lg); Lg = sw32_sum(Lg, Lg);
      if (lane < 8) ((LAS float*)(L + L_FAC))[h * 4 + ww] = __expf(ml[0] - Mg) / Lg;
    }
  }
  BARRIER();
  STAMP(6);
  f32x2 o2;
  {
    const int h = ht >> 7, p = ht & 127;
    const f32x4 fac = *(const LAS f32x4*)(L + L_FAC + h * 16);
    const LAS float* ar = (const LAS float*)(L + L_ACC) + h * 256 + 2 * p;
    f32x2 O = f32x2{fac[0], fac[0]} * *(const LAS f32x2*)ar;
#pragma unroll
    for (int k = 1; k < 4; ++k) O = __builtin_elementwise_fma(f32x2{fac[k], fac[k]}, *(const LAS f32x2*)(ar + k * 512), O);
    o2 = *(const LAS f32x2*)(L + L_FFN + (ht < 224 ? ht : 0) * 8);
    const u32x4 gr = {tag, __float_as_uint(O[0]), tag, __float_as_uint(O[1])};
    __builtin_amdgcn_raw_buffer_store_b128(gr, rws, WS_X1 + (g * 32 + s) * 4096 + ht * 16, 0, SAUX);
  }
  if (DRAIN1) asm volatile("s_waitcnt vmcnt(0)" ::: "memory");
  if (DELAY1) __builtin_amdgcn_s_sleep(DELAY1);
  STAMP(7);
  {
    const int pp = lane & 7, sq = lane >> 3;
    const unsigned ao = WS_X1 + (g * 32 + sq) * 4096 + s * 128 + pp * 16;
    u32x4 go[4]; bool ok[4] = {false, false, false, false};
#pragma unroll
    for (int k = 0; k < 4; ++k) go[k] = u32x4{0, 0, 0, 0};
    for (unsigned spin = 0; spin < SPIN_CAP; ++spin) {
      asm volatile("" ::: "memory");
#pragma unroll
      for (int k = 0; k < 4; ++k) if (!ok[k]) go[k] = __builtin_amdgcn_raw_buffer_load_b128(rws, ao + k * 8 * 4096, 0, 16);
#pragma unroll
      for (int k = 0; k < 4; ++k) ok[k] = tag_ok(go[k], tag);
      if (__all(ok[0] && ok[1] && ok[2] && ok[3])) break;
      if (SLEEP1) __builtin_amdgcn_s_sleep(SLEEP1);
    }
    STAMP(8);
    float a0 = (__uint_as_float(go[0][1]) + __uint_as_float(go[1][1])) + (__uint_as_float(go[2][1]) + __uint_as_float(go[3][1]));
    float a1 = (__uint_as_float(go[0][3]) + __uint_as_float(go[1][3])) + (__uint_as_float(go[2][3]) + __uint_as_float(go[3][3]));
    a0 += dpp<XOR8>(a0); a1 += dpp<XOR8>(a1);
    { const float t = sw16_sum(a0, a1);
      const float t2 = rbf(sw32_sum(t, t));
#pragma unroll
      for (int r = 0; r < 16; ++r) { const float orr = rdlane(t2, (r & 1) * 16 + (r >> 1)); o2 = __builtin_elementwise_fma(f32x2{orr, orr}, wo2[r], o2); } }
    const u32x4 gr = {tag, __float_as_uint(o2[0]), tag, __float_as_uint(o2[1])};
    __builtin_amdgcn_raw_buffer_store_b128(gr, rws, ht < 224 ? WS_X2 + b * 3584 + ht * 16 : 0x7ffffff0u, 0, SAUX);
  }
  if (DRAIN2) asm volatile("s_waitcnt vmcnt(0)" ::: "memory");
  if (DELAY2) __builtin_amdgcn_s_sleep(DELAY2);
  STAMP(9);
  if (b < 56) {
    const int pos = ht & 3, sr = ht >> 2;
    u32x4 gv[4]; bool ok[4] = {false, false, false, false};
#pragma unroll
    for (int k = 0; k < 4; ++k) gv[k] = u32x4{0, 0, 0, 0};
    for (unsigned spin = 0; spin < SPIN_CAP; ++spin) {
      asm volatile("" ::: "memory");
#pragma unroll
      for (int k = 0; k < 4; ++k) if (!ok[k]) gv[k] = __builtin_amdgcn_raw_buffer_load_b128(rws, WS_X2 + (sr + 64 * k) * 3584 + b * 64 + pos * 16, 0, 16);
#pragma unroll
      for (int k = 0; k < 4; ++k) ok[k] = tag_ok(gv[k], tag);
      if (__all(ok[0] && ok[1] && ok[2] && ok[3])) break;
    }
    STAMP(10);
    float a0 = (__uint_as_float(gv[0][1]) + __uint_as_float(gv[1][1])) + (__uint_as_float(gv[2][1]) + __uint_as_float(gv[3][1]));
    float a1 = (__uint_as_float(gv[0][3]) + __uint_as_float(gv[1][3])) + (__uint_as_float(gv[2][3]) + __uint_as_float(gv[3][3]));
    a0 += xor4(a0); a1 += xor4(a1); a0 += dpp<XOR8>(a0); a1 += dpp<XOR8>(a1);
    { const float t = sw16_sum(a0, a1); const float t2 = sw32_sum(t, t);
      LAS float* fin = (LAS float*)(L + L_FIN);
      if ((lane & 0x2c) == 0) fin[hw * 8 + 2 * (lane & 3) + (lane >> 4)] = t2;
      BARRIER();
      if (ht < 8) {
        const float r = xres + ((fin[ht] + fin[8 + ht]) + (fin[16 + ht] + fin[24 + ht]));
        ((GAS unsigned short*)a.out)[b * 8 + ht] = __builtin_bit_cast(unsigned short, (__bf16)r);
      } }
    if (b == 0 && ht == 0) __builtin_amdgcn_raw_buffer_store_b32(tag, rws, WS_EPOCH, 0, 16);
  }
  if (b < 56) pace(tfix0 + TFIX);
}

extern "C" void kernel_launch(void* const* d_in, const int* in_sizes, int n_in, void* d_out, int out_size, void* d_ws, size_t ws_size, hipStream_t stream) {
  Args a;
  a.x = (const unsigned short*)d_in[0]; a.kc = (const unsigned short*)d_in[1]; a.vc = (const unsigned short*)d_in[2];
  a.wq = (const unsigned short*)d_in[3]; a.wo = (const unsigned short*)d_in[4]; a.w1 = (const unsigned short*)d_in[6]; a.w2 = (const unsigned short*)d_in[7];
  a.out = (unsigned short*)d_out; a.ws = (unsigned char*)d_ws;
  (void)hipFuncSetAttribute((const void*)fused_decode, hipFuncAttributeMaxDynamicSharedMemorySize, LDS_BYTES);
  hipLaunchKernelGGL(fused_decode, dim3(NWG), dim3(NTHR), LDS_BYTES, stream, a);
}
```

```cpp
#define TFIX 6000
#include <hip/hip_runtime.h>
#include <stdint.h>

typedef unsigned u32x4 __attribute__((ext_vector_type(4)));
typedef unsigned u32x2 __attribute__((ext_vector_type(2)));
typedef float f32x4 __attribute__((ext_vector_type(4)));
typedef float f32x2 __attribute__((ext_vector_type(2)));
typedef __bf16 bf16x2 __attribute__((ext_vector_type(2)));
#define LAS __attribute__((address_space(3)))
#define GAS __attribute__((address_space(1)))

#define P0 6
#define ROT 1
#define WAUX 2
#define STAMPS 0
#define NOQ 0
#define NOCK 0
#define NOCV 0
#define NOW_ 0
#define SW 0
#define DRAIN 0
#define QSLEEP 2
#define MLSLEEP 16
#define NOML 0
#define AUXQ WAUX
#define AUX1 WAUX
#define TAU 3790
#define TOFF 280
#define PG 4
#define DRAIN1 1
#define DRAIN2 1
#define DELAY1 0
#define DELAY2 0
#define SAUX 16
#define SLEEP1 1

constexpr int D = 448, SK = 32768, DH = 256, DFF = 1792, NQ = 4096;
#define WIN 16
constexpr int NWG = 256, NTHR = 512, W = WIN, NIT = 128 / W, NG = W / 16;
constexpr float EPS = 1e-5f;
constexpr unsigned SPIN_CAP = 1u << 17;
constexpr unsigned WS_EPOCH = 0, WS_ML = 4096, WS_XQ = 16384, WS_XH = WS_XQ + 131072, WS_X1 = 524288, WS_X2 = WS_X1 + (1u << 20), WS_STAMP = 3u << 20, WS_USED = 4u << 20;
static_assert(WS_ML + 8192 <= WS_XQ && WS_XH + 224 * 512 <= WS_X1 && WS_X2 + 256 * 3584 <= WS_STAMP, "ws");
constexpr unsigned L_Q = 0;
constexpr unsigned L_XN = 1024;
constexpr unsigned L_QP = L_XN + 4 * 448 * 4;
constexpr unsigned L_CNT = L_QP + 1024;
constexpr unsigned L_MLW = L_CNT + 16;
constexpr unsigned L_FAC = L_MLW + 64;
constexpr unsigned L_FFN = L_FAC + 32;
constexpr unsigned L_FIN = L_FFN + 448 * 4;
constexpr unsigned L_P = L_FIN + 128;
constexpr unsigned L_ACC = L_P + 8192;
constexpr unsigned L_END = L_ACC + 8192;
#define LDSKB 88
constexpr unsigned LDS_BYTES = LDSKB * 1024;
static_assert(L_END <= LDS_BYTES && L_P % 16 == 0 && L_FFN % 16 == 0 && L_FAC % 16 == 0 && L_MLW % 16 == 0, "lds");

__device__ __forceinline__ float bflo(unsigned u) { return __builtin_bit_cast(float, u << 16); }
__device__ __forceinline__ float bfhi(unsigned u) { return __builtin_bit_cast(float, u & 0xffff0000u); }
__device__ __forceinline__ float rbf(float f) { return (float)(__bf16)f; }
__device__ __forceinline__ unsigned pk2(float lo, float hi) { bf16x2 v = {(__bf16)lo, (__bf16)hi}; return __builtin_bit_cast(unsigned, v); }
__device__ __forceinline__ float dot2(unsigned a, unsigned b, float c) {
  return __builtin_amdgcn_fdot2_f32_bf16(__builtin_bit_cast(bf16x2, a), __builtin_bit_cast(bf16x2, b), c, false);
}
template <int CTRL> __device__ __forceinline__ float dpp(float x) {
  return __builtin_bit_cast(float, __builtin_amdgcn_mov_dpp(__builtin_bit_cast(int, x), CTRL, 0xf, 0xf, true));
}
constexpr int XOR1 = 0xB1, XOR2 = 0x4E, XOR3 = 0x1B, XOR7 = 0x141, XOR8 = 0x128, ROR4 = 0x124;
__device__ __forceinline__ float xor4(float x) { return dpp<XOR3>(dpp<XOR7>(x)); }
__device__ __forceinline__ float sw16_sum(float a, float b) {
  auto r = __builtin_amdgcn_permlane16_swap(__float_as_uint(a), __float_as_uint(b), false, false);
  return __uint_as_float(r[0]) + __uint_as_float(r[1]);
}
__device__ __forceinline__ float sw32_sum(float a, float b) {
  auto r = __builtin_amdgcn_permlane32_swap(__float_as_uint(a), __float_as_uint(b), false, false);
  return __uint_as_float(r[0]) + __uint_as_float(r[1]);
}
__device__ __forceinline__ float sw16_max(float a) {
  auto r = __builtin_amdgcn_permlane16_swap(__float_as_uint(a), __float_as_uint(a), false, false);
  return fmaxf(__uint_as_float(r[0]), __uint_as_float(r[1]));
}
__device__ __forceinline__ float sw32_max(float a) {
  auto r = __builtin_amdgcn_permlane32_swap(__float_as_uint(a), __float_as_uint(a), false, false);
  return fmaxf(__uint_as_float(r[0]), __uint_as_float(r[1]));
}
__device__ __forceinline__ float rdlane(float v, int l) { return __builtin_bit_cast(float, __builtin_amdgcn_readlane(__builtin_bit_cast(int, v), l)); }
#define BARRIER() do { __builtin_amdgcn_sched_barrier(0); asm volatile("s_waitcnt lgkmcnt(0)\n\ts_barrier" ::: "memory"); __builtin_amdgcn_sched_barrier(0); } while (0)
#define NOW() __builtin_amdgcn_s_memrealtime()
__device__ __forceinline__ void pace(unsigned tgt) {
  asm volatile("pace_%=:\n\ts_memrealtime s[60:61]\n\ts_waitcnt lgkmcnt(0)\n\ts_sub_u32 s60, s60, %0\n\ts_cmp_lt_i32 s60, 0\n\ts_cbranch_scc1 pace_%=" :: "s"(tgt) : "memory", "scc", "s60", "s61");
}
__device__ __forceinline__ unsigned lds_ld(LAS unsigned* p) { return __hip_atomic_load(p, __ATOMIC_RELAXED, __HIP_MEMORY_SCOPE_WORKGROUP); }
__device__ __forceinline__ void lds_inc(LAS unsigned* p) { (void)__hip_atomic_fetch_add(p, 1u, __ATOMIC_RELAXED, __HIP_MEMORY_SCOPE_WORKGROUP); }
__device__ __forceinline__ bool tag_ok(const u32x4& v, unsigned tag) { return v[0] == tag && v[2] == tag; }

struct Args {
  const unsigned short *x, *kc, *vc, *wq, *wo, *w1, *w2;
  unsigned short* out;
  unsigned char* ws;
};

__global__ void __launch_bounds__(NTHR, 2) fused_decode(Args a) {
  unsigned long long tfix64; asm volatile("s_memrealtime %0\n\ts_waitcnt lgkmcnt(0)" : "=s"(tfix64) :: "memory");
  __builtin_amdgcn_sched_barrier(0);
  const unsigned tfix0 = (unsigned)tfix64;
  extern __shared__ __attribute__((aligned(16))) unsigned char smem[];
  LAS unsigned char* L = (LAS unsigned char*)smem;
  const int tid = threadIdx.x, lane = tid & 63, w = __builtin_amdgcn_readfirstlane(tid >> 6);
  const int b = blockIdx.x, g = b & 7, s = b >> 3;
#define STAMP(i)

  if (w < 4) {
    const int hf = lane >> 5, c = lane & 31;
    const unsigned voff = lane * 16;
    const unsigned sbase = (unsigned)s * (1024u * 512u) + (unsigned)w * 2048u;
    const int rp = (s * ROT) & 63;
    const unsigned short* kh = a.kc + (size_t)g * SK * DH;
    const unsigned short* vh = a.vc + (size_t)g * SK * DH;
    constexpr int HEADB = SK * DH * 2;
    auto soff = [&](int p) -> unsigned { return sbase + ((unsigned)((p + rp) & 63) << 13); };
    u32x4 buf[W];
    const unsigned tb = (unsigned)NOW() + TOFF;
    BARRIER();
    {
      const __amdgpu_buffer_rsrc_t rk = __builtin_amdgcn_make_buffer_rsrc((void*)kh, 0, HEADB, 0x00020000);
#pragma unroll
      for (int i = 0; i < W; ++i) {
        if (i >= P0) asm volatile("s_waitcnt vmcnt(%0)" ::"i"(P0 - 1) : "memory");
        buf[i] = __builtin_amdgcn_raw_buffer_load_b128(rk, voff + (i & 1) * 1024, soff(i >> 1), 2);
        __builtin_amdgcn_sched_barrier(0);
      }
    }
    BARRIER();
    const u32x4 q0 = *(const LAS u32x4*)(L + L_Q + c * 16), q1 = *(const LAS u32x4*)(L + L_Q + 512 + c * 16);
    LAS float* pl = (LAS float*)(L + L_P) + w * 512 + hf * 256;
    float m = -INFINITY;
#pragma unroll 1
    for (int it = 0; it < NIT; ++it) {
      const bool tok = it < NIT - 1;
      const __amdgpu_buffer_rsrc_t rn = __builtin_amdgcn_make_buffer_rsrc((void*)(tok ? kh : vh), 0, HEADB, 0x00020000);
      const int pb = tok ? (it + 1) * (W / 2) : 0;
#pragma unroll
      for (int gr = 0; gr < NG; ++gr) {
        float x[32];
#pragma unroll
        for (int i = 0; i < 16; ++i) {
          const int idx = gr * 16 + i;
          float s0 = 0.f, s1 = 0.f;
#pragma unroll
          for (int k = 0; k < 4; ++k) { s0 = dot2(buf[idx][k], q0[k], s0); s1 = dot2(buf[idx][k], q1[k], s1); }
          x[2 * i] = s0; x[2 * i + 1] = s1;
          if (TAU && (idx % PG) == 0) { asm volatile("" : "+v"(x[2 * i]), "+v"(x[2 * i + 1]) :: "memory"); pace(tb + (((unsigned)(it * W + idx) * TAU) >> 8)); }
          buf[idx] = __builtin_amdgcn_raw_buffer_load_b128(rn, voff + (idx & 1) * 1024, soff(pb + (idx >> 1)), 2);
          __builtin_amdgcn_sched_barrier(0);
        }
#pragma unroll
        for (int hh = 0; hh < 16; ++hh) x[hh] = sw16_sum(x[hh], x[hh + 16]);
#define TR(HALF, CTRL, BIT) _Pragma("unroll") for (int hh = 0; hh < (HALF); ++hh) { \
          const float keep = (c & (BIT)) ? x[hh + (HALF)] : x[hh], send = (c & (BIT)) ? x[hh] : x[hh + (HALF)]; x[hh] = keep + dpp<CTRL>(send); }
        TR(8, XOR8, 8) TR(4, XOR7, 4) TR(2, XOR2, 2) TR(1, XOR1, 1)
#undef TR
        const float sv = rbf(x[0]) * 0.0625f;
        m = fmaxf(m, sv);
        pl[(it * NG + gr) * 32 + c] = sv;
        __builtin_amdgcn_sched_barrier(0);
      }
    }
    STAMP(4);
    m = fmaxf(m, dpp<XOR2>(m)); m = fmaxf(m, dpp<ROR4>(m)); m = fmaxf(m, dpp<XOR8>(m)); m = sw16_max(m); m = sw32_max(m);
    float l = 0.f;
    {
      float sc[8];
#pragma unroll
      for (int j = 0; j < 8; ++j) sc[j] = pl[j * 32 + c];
#pragma unroll
      for (int j = 0; j < 8; ++j) { const float p = __expf(sc[j] - m); l += p; pl[j * 32 + c] = p; }
    }
    l += dpp<XOR2>(l); l += dpp<ROR4>(l); l += dpp<XOR8>(l); l = sw16_sum(l, l); l = sw32_sum(l, l);
    if (lane < 2) *(LAS f32x2*)(L + L_MLW + (w * 2 + lane) * 8) = f32x2{m, l};
    if (lane == 0) lds_inc((LAS unsigned*)(L + L_CNT) + 1);
    asm volatile("s_waitcnt lgkmcnt(0)" ::: "memory");
    f32x2 ac2[8];
#pragma unroll
    for (int k = 0; k < 8; ++k) ac2[k] = f32x2{0.f, 0.f};
#pragma unroll 1
    for (int it = 0; it < NIT; ++it) {
      const __amdgpu_buffer_rsrc_t rn = __builtin_amdgcn_make_buffer_rsrc((void*)vh, 0, it < NIT - 1 ? HEADB : 0, 0x00020000);
      const int pb = (it + 1) * (W / 2);
      const unsigned tv = tb + ((((unsigned)(128 + it * W) * TAU) >> 8) & (it < NIT - 1 ? ~0u : 0u));
      f32x4 pp = *(const LAS f32x4*)(pl + it * W * 2);
#pragma unroll
      for (int i = 0; i < W; i += 2) {
        const f32x4 pc = pp;
        if (i + 2 < W) pp = *(const LAS f32x4*)(pl + (it * W + i + 2) * 2);
#pragma unroll
        for (int u = 0; u < 2; ++u) {
          const f32x2 pa = {pc[2 * u], pc[2 * u]}, pb2 = {pc[2 * u + 1], pc[2 * u + 1]};
#pragma unroll
          for (int k = 0; k < (NOCV ? 1 : 4); ++k) {
            const f32x2 vv = NOCV ? f32x2{__uint_as_float(buf[i + u][0] ^ buf[i + u][1]), __uint_as_float(buf[i + u][2] ^ buf[i + u][3])} : f32x2{bflo(buf[i + u][k]), bfhi(buf[i + u][k])};
            ac2[k] = __builtin_elementwise_fma(pa, vv, ac2[k]);
            ac2[4 + k] = __builtin_elementwise_fma(pb2, vv, ac2[4 + k]);
          }
          asm volatile("" : "+v"(ac2[0]), "+v"(ac2[1]), "+v"(ac2[2]), "+v"(ac2[3]), "+v"(ac2[4]), "+v"(ac2[5]), "+v"(ac2[6]), "+v"(ac2[7]) :: "memory");
          if (TAU && ((i + u) % PG) == 0) pace(tv + (((unsigned)(i + u) * TAU) >> 8));
          buf[i + u] = __builtin_amdgcn_raw_buffer_load_b128(rn, voff + u * 1024, soff(pb + (i >> 1)), 2);
          __builtin_amdgcn_sched_barrier(0);
        }
      }
    }
    STAMP(5);
    {
      float acc[8];
#pragma unroll
      for (int k = 0; k < 4; ++k) { acc[2 * k] = sw32_sum(ac2[k][0], ac2[4 + k][0]); acc[2 * k + 1] = sw32_sum(ac2[k][1], ac2[4 + k][1]); }
      LAS float* rec = (LAS float*)(L + L_ACC) + (w * 2 + hf) * 256 + 8 * c;
      *(LAS f32x4*)rec = f32x4{acc[0], acc[1], acc[2], acc[3]};
      *(LAS f32x4*)(rec + 4) = f32x4{acc[4], acc[5], acc[6], acc[7]};
    }
    BARRIER();
    return;
  }

  STAMP(0);

  const int hw = w - 4, ht = tid - 256;
  const __amdgpu_buffer_rsrc_t rws = __builtin_amdgcn_make_buffer_rsrc((void*)a.ws, 0, WS_USED, 0x00020000);
  LAS unsigned* cnt = (LAS unsigned*)(L + L_CNT);
  LAS float* xn = (LAS float*)(L + L_XN) + hw * 448;
  unsigned tag;
  {
    const int cb = s >> 2, kq = s & 3;
    const __amdgpu_buffer_rsrc_t rx = __builtin_amdgcn_make_buffer_rsrc((void*)a.x, 0, D * 2, 0x00020000);
    const __amdgpu_buffer_rsrc_t rq = __builtin_amdgcn_make_buffer_rsrc((void*)(a.wq + (size_t)kq * 112 * NQ), 0, (NOW_ & 1) ? 0 : 112 * NQ * 2, 0x00020000);
    const u32x4 xv = __builtin_amdgcn_raw_buffer_load_b128(rx, lane * 16, 0, 0);
    const int cg = ht & 7, rs = ht >> 3;
    u32x4 wv[4];
    const unsigned qo = (unsigned)rs * (NQ * 2) + (unsigned)(g * 512 + cb * 64 + cg * 8) * 2;
#pragma unroll
    for (int i = 0; i < 4; ++i) wv[i] = __builtin_amdgcn_raw_buffer_load_b128(rq, qo + i * 32 * (NQ * 2), 0, WAUX);
    const unsigned ep = __builtin_amdgcn_raw_buffer_load_b32(rws, 0, WS_EPOCH, 16);
    if (ht < 2) cnt[ht] = 0u;
    BARRIER();
    STAMP(12);
    float f[8];
#pragma unroll
    for (int i = 0; i < 4; ++i) { f[2 * i] = bflo(xv[i]); f[2 * i + 1] = bfhi(xv[i]); }
    const float x0 = rdlane(f[0], 0);
    float s1 = 0.f, s2 = 0.f;
#pragma unroll
    for (int i = 0; i < 8; ++i) { const float d = f[i] - x0; s1 += d; s2 = fmaf(d, d, s2); }
    if (lane >= 56) { s1 = 0.f; s2 = 0.f; }
    s1 += dpp<XOR1>(s1); s2 += dpp<XOR1>(s2); s1 += dpp<XOR2>(s1); s2 += dpp<XOR2>(s2);
    s1 += dpp<XOR7>(s1); s2 += dpp<XOR7>(s2); s1 += dpp<XOR8>(s1); s2 += dpp<XOR8>(s2);
    { const float t = sw16_sum(s1, s2);
      const float t2 = sw32_sum(t, t); s1 = rdlane(t2, 0); s2 = rdlane(t2, 16); }
    const float md = s1 * (1.0f / D);
    const float var = fmaxf(fmaf(-md, md, s2 * (1.0f / D)), 0.f);
    const float mb = rbf(x0 + md), den = rbf(sqrtf(rbf(rbf(var) + EPS)));
    const float rden = 1.0f / den;
    if (lane < 56) {
      f32x4 n0, n1;
#pragma unroll
      for (int i = 0; i < 4; ++i) { n0[i] = rbf(rbf(f[i] - mb) * rden); n1[i] = rbf(rbf(f[4 + i] - mb) * rden); }
      *(LAS f32x4*)(xn + lane * 8) = n0; *(LAS f32x4*)(xn + lane * 8 + 4) = n1;
    }
    asm volatile("" ::: "memory");
    STAMP(14);
    float xr[4];
#pragma unroll
    for (int i = 0; i < 4; ++i) { const int row = rs + 32 * i; xr[i] = xn[kq * 112 + (row < 112 ? row : 0)]; }
    float qa[8];
#pragma unroll
    for (int k = 0; k < 8; ++k) qa[k] = 0.f;
#pragma unroll
    for (int i = 0; i < 4; ++i) {
#pragma unroll
      for (int k = 0; k < 4; ++k) { qa[2 * k] = fmaf(xr[i], bflo(wv[i][k]), qa[2 * k]); qa[2 * k + 1] = fmaf(xr[i], bfhi(wv[i][k]), qa[2 * k + 1]); }
    }
#pragma unroll
    for (int k = 0; k < 4; ++k) qa[k] = sw32_sum(qa[k], qa[k + 4]);
#pragma unroll
    for (int k = 0; k < 2; ++k) qa[k] = sw16_sum(qa[k], qa[k + 2]);
    { const float keep = (lane & 8) ? qa[1] : qa[0], send = (lane & 8) ? qa[0] : qa[1]; qa[0] = keep + dpp<XOR8>(send); }
    LAS float* qp = (LAS float*)(L + L_QP);
    qp[hw * 64 + cg * 8 + (lane >> 5) * 4 + ((lane >> 4) & 1) * 2 + ((lane >> 3) & 1)] = qa[0];
    if (lane == 0) lds_inc(cnt);
    STAMP(1);
    tag = __builtin_amdgcn_readfirstlane(ep) + 1u;
    for (unsigned spin = 0; spin < SPIN_CAP; ++spin) { if (__builtin_amdgcn_readfirstlane(lds_ld(cnt)) == 4u) break; }
    asm volatile("" ::: "memory");
    const int pr = ht & 31, cbt = ht >> 5;
    f32x2 own = *(const LAS f32x2*)(qp + 2 * pr);
#pragma unroll
    for (int k = 1; k < 4; ++k) own += *(const LAS f32x2*)(qp + k * 64 + 2 * pr);
    if (cbt == 2 * hw && (lane >> 3) == hw)
      __builtin_amdgcn_raw_buffer_store_b128(u32x4{tag, __float_as_uint(own[0]), tag, __float_as_uint(own[1])}, rws, WS_XQ + ((g * 8 + cb) * 4 + kq) * 512 + pr * 16, 0, 16);
    STAMP(2);
    u32x4 gq[4]; bool ok[4];
#pragma unroll
    for (int k = 0; k < 4; ++k) { ok[k] = cbt == cb && k == kq; gq[k] = u32x4{tag, __float_as_uint(own[0]), tag, __float_as_uint(own[1])}; }
    for (unsigned spin = 0; spin < SPIN_CAP; ++spin) {
      asm volatile("" ::: "memory");
#pragma unroll
      for (int k = 0; k < 4; ++k) if (!ok[k]) gq[k] = __builtin_amdgcn_raw_buffer_load_b128(rws, WS_XQ + ((g * 8 + cbt) * 4 + k) * 512 + pr * 16, 0, 16);
#pragma unroll
      for (int k = 0; k < 4; ++k) ok[k] = tag_ok(gq[k], tag);
      if (__all(ok[0] && ok[1] && ok[2] && ok[3])) break;
      if (QSLEEP) __builtin_amdgcn_s_sleep(QSLEEP);
    }
    { const float q0 = (__uint_as_float(gq[0][1]) + __uint_as_float(gq[1][1])) + (__uint_as_float(gq[2][1]) + __uint_as_float(gq[3][1]));
      const float q1 = (__uint_as_float(gq[0][3]) + __uint_as_float(gq[1][3])) + (__uint_as_float(gq[2][3]) + __uint_as_float(gq[3][3]));
      *(LAS unsigned*)(L + L_Q + ht * 4) = pk2(q0, q1); }
    STAMP(3);
  }
  BARRIER();
  f32x2 wo2[16];
  float xres = 0.f;
  {
    const __amdgpu_buffer_rsrc_t ro = __builtin_amdgcn_make_buffer_rsrc((void*)a.wo, 0, (NOW_ & 2) ? 0 : NQ * D * 2, 0x00020000);
    const unsigned co = ht < 224 ? ht * 4 : 0x7ffffff0u;
    unsigned wov[16];
#pragma unroll
    for (int r = 0; r < 16; ++r) wov[r] = __builtin_amdgcn_raw_buffer_load_b32(ro, co, (unsigned)(g * 512 + s * 16 + r) * (D * 2), WAUX);
    if (b < 56 && ht < 8) xres = bflo((unsigned)a.x[b * 8 + ht]);
    if (hw == 1) {
      const bool has_ffn = s < 28;
      const int j = g * 28 + s, cbh = j >> 3, e = j & 7;
      const __amdgpu_buffer_rsrc_t r1 = __builtin_amdgcn_make_buffer_rsrc((void*)a.w1, 0, has_ffn && !(NOW_ & 4) ? D * DFF * 2 : 0, 0x00020000);
      const __amdgpu_buffer_rsrc_t r2 = __builtin_amdgcn_make_buffer_rsrc((void*)a.w2, 0, has_ffn && !(NOW_ & 8) ? DFF * D * 2 : 0, 0x00020000);
      const int cg = lane & 7, rs = lane >> 3;
      u32x4 w1v[7], w2v[8];
#pragma unroll
      for (int i = 0; i < 7; ++i) w1v[i] = __builtin_amdgcn_raw_buffer_load_b128(r1, (unsigned)(e * 56 + rs + 8 * i) * (DFF * 2) + (unsigned)(cbh * 64 + cg * 8) * 2, 0, WAUX);
#pragma unroll
      for (int r = 0; r < 8; ++r) w2v[r] = __builtin_amdgcn_raw_buffer_load_b128(r2, lane < 56 ? lane * 16 : 0x7ffffff0u, (unsigned)(j * 8 + r) * (D * 2), WAUX);
      float ha[8];
#pragma unroll
      for (int k = 0; k < 8; ++k) ha[k] = 0.f;
#pragma unroll
      for (int i = 0; i < 7; ++i) { const float xr = xn[(has_ffn ? e * 56 : 0) + rs + 8 * i];
#pragma unroll
        for (int k = 0; k < 4; ++k) { ha[2 * k] = fmaf(xr, bflo(w1v[i][k]), ha[2 * k]); ha[2 * k + 1] = fmaf(xr, bfhi(w1v[i][k]), ha[2 * k + 1]); } }
#pragma unroll
      for (int k = 0; k < 4; ++k) ha[k] = sw32_sum(ha[k], ha[k + 4]);
#pragma unroll
      for (int k = 0; k < 2; ++k) ha[k] = sw16_sum(ha[k], ha[k + 2]);
      { const float keep = (lane & 8) ? ha[1] : ha[0], send = (lane & 8) ? ha[0] : ha[1]; ha[0] = keep + dpp<XOR8>(send); }
      const float hn = dpp<XOR8>(ha[0]);
      float hv0 = 0.f, hv1 = 0.f;
      if (has_ffn) {
        if ((lane & 8) == 0)
          __builtin_amdgcn_raw_buffer_store_b128(u32x4{tag, __float_as_uint(ha[0]), tag, __float_as_uint(hn)}, rws,
                                                 WS_XH + j * 512 + (cg * 4 + (lane >> 5) * 2 + ((lane >> 4) & 1)) * 16, 0, 16);
        asm volatile("s_waitcnt vmcnt(0)" ::: "memory");
        u32x4 gh = {0, 0, 0, 0}; bool ok = lane >= 32;
        for (unsigned spin = 0; spin < SPIN_CAP; ++spin) {
          asm volatile("" ::: "memory");
          if (!ok) { gh = __builtin_amdgcn_raw_buffer_load_b128(rws, WS_XH + (cbh * 8 + (lane >> 2)) * 512 + (e * 4 + (lane & 3)) * 16, 0, 16); ok = tag_ok(gh, tag); }
          if (__all(ok)) break;
          __builtin_amdgcn_s_sleep(16);
        }
        hv0 = lane < 32 ? __uint_as_float(gh[1]) : 0.f; hv1 = lane < 32 ? __uint_as_float(gh[3]) : 0.f;
        hv0 += xor4(hv0); hv1 += xor4(hv1); hv0 += dpp<XOR8>(hv0); hv1 += dpp<XOR8>(hv1);
        hv0 = sw16_sum(hv0, hv0); hv1 = sw16_sum(hv1, hv1);
        hv0 = fmaxf(rbf(hv0), 0.f); hv1 = fmaxf(rbf(hv1), 0.f);
      }
      float fo[8];
#pragma unroll
      for (int k = 0; k < 8; ++k) fo[k] = 0.f;
#pragma unroll
      for (int r = 0; r < 8; ++r) { const float hr = rdlane((r & 1) ? hv1 : hv0, r >> 1);
#pragma unroll
        for (int k = 0; k < 4; ++k) { fo[2 * k] = fmaf(hr, bflo(w2v[r][k]), fo[2 * k]); fo[2 * k + 1] = fmaf(hr, bfhi(w2v[r][k]), fo[2 * k + 1]); } }
      if (lane < 56) { LAS float* ff = (LAS float*)(L + L_FFN) + lane * 8;
        *(LAS f32x4*)ff = f32x4{fo[0], fo[1], fo[2], fo[3]}; *(LAS f32x4*)(ff + 4) = f32x4{fo[4], fo[5], fo[6], fo[7]}; }
    }
#pragma unroll
    for (int r = 0; r < 16; ++r) { wo2[r] = f32x2{bflo(wov[r]), bfhi(wov[r])}; asm volatile("" : "+v"(wo2[r])); }
    asm volatile("" : "+v"(xres));
    if (hw == 0) {
      for (unsigned spin = 0; spin < SPIN_CAP * 4; ++spin) { if (__builtin_amdgcn_readfirstlane(lds_ld(cnt + 1)) == 4u) break; __builtin_amdgcn_s_sleep(8); }
      asm volatile("" ::: "memory");
      const int h = lane & 1, ww = (lane >> 1) & 3;
      const f32x2 ml = *(const LAS f32x2*)(L + L_MLW + (ww * 2 + h) * 8);
      float M = fmaxf(ml[0], dpp<XOR2>(ml[0])); M = fmaxf(M, xor4(M));
      float Ls = __expf(ml[0] - M) * ml[1]; Ls += dpp<XOR2>(Ls); Ls += xor4(Ls);
      if (lane < 2) { const u32x4 gr = {tag, __float_as_uint(M), tag, __float_as_uint(Ls)};
        __builtin_amdgcn_raw_buffer_store_b128(gr, rws, WS_ML + (g * 32 + s) * 32 + lane * 16, 0, 16); }
      asm volatile("s_waitcnt vmcnt(0)" ::: "memory");
      u32x4 gm = {0, 0, 0, 0}; bool ok = false;
      for (unsigned spin = 0; spin < SPIN_CAP * 4; ++spin) {
        asm volatile("" ::: "memory");
        if (!ok) gm = __builtin_amdgcn_raw_buffer_load_b128(rws, WS_ML + g * 1024 + lane * 16, 0, 16);
        ok = tag_ok(gm, tag);
        if (__all(ok)) break;
        __builtin_amdgcn_s_sleep(MLSLEEP);
      }
      const float ms = __uint_as_float(gm[1]);
      float Mg = fmaxf(ms, dpp<XOR2>(ms)); Mg = fmaxf(Mg, xor4(Mg)); Mg = fmaxf(Mg, dpp<XOR8>(Mg)); Mg = sw16_max(Mg); Mg = sw32_max(Mg);
      float Lg = __expf(ms - Mg) * __uint_as_float(gm[3]);
      Lg += dpp<XOR2>(Lg); Lg += xor4(Lg); Lg += dpp<XOR8>(Lg); Lg = sw16_sum(Lg, Lg); Lg = sw32_sum(Lg, Lg);
      if (lane < 8) ((LAS float*)(L + L_FAC))[h * 4 + ww] = __expf(ml[0] - Mg) / Lg;
    }
  }
  BARRIER();
  STAMP(6);
  f32x2 o2;
  {
    const int h = ht >> 7, p = ht & 127;
    const f32x4 fac = *(const LAS f32x4*)(L + L_FAC + h * 16);
    const LAS float* ar = (const LAS float*)(L + L_ACC) + h * 256 + 2 * p;
    f32x2 O = f32x2{fac[0], fac[0]} * *(const LAS f32x2*)ar;
#pragma unroll
    for (int k = 1; k < 4; ++k) O = __builtin_elementwise_fma(f32x2{fac[k], fac[k]}, *(const LAS f32x2*)(ar + k * 512), O);
    o2 = *(const LAS f32x2*)(L + L_FFN + (ht < 224 ? ht : 0) * 8);
    const u32x4 gr = {tag, __float_as_uint(O[0]), tag, __float_as_uint(O[1])};
    __builtin_amdgcn_raw_buffer_store_b128(gr, rws, WS_X1 + (g * 32 + s) * 4096 + ht * 16, 0, SAUX);
  }
  if (DRAIN1) asm volatile("s_waitcnt vmcnt(0)" ::: "memory");
  if (DELAY1) __builtin_amdgcn_s_sleep(DELAY1);
  STAMP(7);
  {
    const int pp = lane & 7, sq = lane >> 3;
    const unsigned ao = WS_X1 + (g * 32 + sq) * 4096 + s * 128 + pp * 16;
    u32x4 go[4]; bool ok[4] = {false, false, false, false};
#pragma unroll
    for (int k = 0; k < 4; ++k) go[k] = u32x4{0, 0, 0, 0};
    for (unsigned spin = 0; spin < SPIN_CAP; ++spin) {
      asm volatile("" ::: "memory");
#pragma unroll
      for (int k = 0; k < 4; ++k) if (!ok[k]) go[k] = __builtin_amdgcn_raw_buffer_load_b128(rws, ao + k * 8 * 4096, 0, 16);
#pragma unroll
      for (int k = 0; k < 4; ++k) ok[k] = tag_ok(go[k], tag);
      if (__all(ok[0] && ok[1] && ok[2] && ok[3])) break;
      if (SLEEP1) __builtin_amdgcn_s_sleep(SLEEP1);
    }
    STAMP(8);
    float a0 = (__uint_as_float(go[0][1]) + __uint_as_float(go[1][1])) + (__uint_as_float(go[2][1]) + __uint_as_float(go[3][1]));
    float a1 = (__uint_as_float(go[0][3]) + __uint_as_float(go[1][3])) + (__uint_as_float(go[2][3]) + __uint_as_float(go[3][3]));
    a0 += dpp<XOR8>(a0); a1 += dpp<XOR8>(a1);
    { const float t = sw16_sum(a0, a1);
      const float t2 = rbf(sw32_sum(t, t));
#pragma unroll
      for (int r = 0; r < 16; ++r) { const float orr = rdlane(t2, (r & 1) * 16 + (r >> 1)); o2 = __builtin_elementwise_fma(f32x2{orr, orr}, wo2[r], o2); } }
    const u32x4 gr = {tag, __float_as_uint(o2[0]), tag, __float_as_uint(o2[1])};
    __builtin_amdgcn_raw_buffer_store_b128(gr, rws, ht < 224 ? WS_X2 + b * 3584 + ht * 16 : 0x7ffffff0u, 0, SAUX);
  }
  if (DRAIN2) asm volatile("s_waitcnt vmcnt(0)" ::: "memory");
  if (DELAY2) __builtin_amdgcn_s_sleep(DELAY2);
  STAMP(9);
  if (b < 56) {
    const int pos = ht & 3, sr = ht >> 2;
    u32x4 gv[4]; bool ok[4] = {false, false, false, false};
#pragma unroll
    for (int k = 0; k < 4; ++k) gv[k] = u32x4{0, 0, 0, 0};
    for (unsigned spin = 0; spin < SPIN_CAP; ++spin) {
      asm volatile("" ::: "memory");
#pragma unroll
      for (int k = 0; k < 4; ++k) if (!ok[k]) gv[k] = __builtin_amdgcn_raw_buffer_load_b128(rws, WS_X2 + (sr + 64 * k) * 3584 + b * 64 + pos * 16, 0, 16);
#pragma unroll
      for (int k = 0; k < 4; ++k) ok[k] = tag_ok(gv[k], tag);
      if (__all(ok[0] && ok[1] && ok[2] && ok[3])) break;
    }
    STAMP(10);
    float a0 = (__uint_as_float(gv[0][1]) + __uint_as_float(gv[1][1])) + (__uint_as_float(gv[2][1]) + __uint_as_float(gv[3][1]));
    float a1 = (__uint_as_float(gv[0][3]) + __uint_as_float(gv[1][3])) + (__uint_as_float(gv[2][3]) + __uint_as_float(gv[3][3]));
    a0 += xor4(a0); a1 += xor4(a1); a0 += dpp<XOR8>(a0); a1 += dpp<XOR8>(a1);
    { const float t = sw16_sum(a0, a1); const float t2 = sw32_sum(t, t);
      LAS float* fin = (LAS float*)(L + L_FIN);
      if ((lane & 0x2c) == 0) fin[hw * 8 + 2 * (lane & 3) + (lane >> 4)] = t2;
      BARRIER();
      if (ht < 8) {
        const float r = xres + ((fin[ht] + fin[8 + ht]) + (fin[16 + ht] + fin[24 + ht]));
        ((GAS unsigned short*)a.out)[b * 8 + ht] = __builtin_bit_cast(unsigned short, (__bf16)r);
      } }
    if (b == 0 && ht == 0) __builtin_amdgcn_raw_buffer_store_b32(tag, rws, WS_EPOCH, 0, 16);
  }
  if (b < 56) pace(tfix0 + TFIX);
}

extern "C" void kernel_launch(void* const* d_in, const int* in_sizes, int n_in, void* d_out, int out_size, void* d_ws, size_t ws_size, hipStream_t stream) {
  Args a;
  a.x = (const unsigned short*)d_in[0]; a.kc = (const unsigned short*)d_in[1]; a.vc = (const unsigned short*)d_in[2];
  a.wq = (const unsigned short*)d_in[3]; a.wo = (const unsigned short*)d_in[4]; a.w1 = (const unsigned short*)d_in[6]; a.w2 = (const unsigned short*)d_in[7];
  a.out = (unsigned short*)d_out; a.ws = (unsigned char*)d_ws;
  (void)hipFuncSetAttribute((const void*)fused_decode, hipFuncAttributeMaxDynamicSharedMemorySize, LDS_BYTES);
  hipLaunchKernelGGL(fused_decode, dim3(NWG), dim3(NTHR), LDS_BYTES, stream, a);
}
```

```cpp
#include <hip/hip_runtime.h>
#include <stdint.h>

typedef unsigned u32x4 __attribute__((ext_vector_type(4)));
typedef float f32x4 __attribute__((ext_vector_type(4)));
typedef float f32x2 __attribute__((ext_vector_type(2)));
typedef __bf16 bf16x2 __attribute__((ext_vector_type(2)));
#define LAS __attribute__((address_space(3)))
#define GAS __attribute__((address_space(1)))

constexpr int RG = 2;
constexpr int P0 = 5;
constexpr unsigned TAU = 3710;
constexpr unsigned TOFF = 320;
constexpr unsigned TGAP = 30;
constexpr int DELAY1 = 12, DELAY2 = 13;
constexpr unsigned MLG = 3000;
constexpr unsigned FFG = 1200;
constexpr int GP = 4;

constexpr int D = 448, SK = 32768, DH = 256, DFF = 1792, NQ = 4096;
constexpr int NWG = 256, NTHR = 512, W = RG * 8;
constexpr int HEADB = SK * DH * 2;
constexpr float EPS = 1e-5f;
constexpr unsigned SPIN_CAP = 1u << 18;
constexpr unsigned OOB = 0x7ffffff0u;
static_assert(P0 < W && RG >= 2 && RG <= 4, "the wait in the ring's prologue also covers the weights' LDS-DMA");
constexpr unsigned WS_EPOCH = 0;
constexpr unsigned WS_ML = 4096;
constexpr unsigned WS_XT = 12288;
constexpr unsigned WS_XQ = 16384;
constexpr unsigned WS_XH = WS_XQ + 131072;
constexpr unsigned WS_X1 = 524288;
constexpr unsigned WS_X2 = WS_X1 + (1u << 20);
constexpr unsigned WS_USED = WS_X2 + 56 * 16384;
static_assert(WS_ML + 8192 <= WS_XT && WS_XT + 1024 <= WS_XQ && WS_XH + 224 * 512 <= WS_X1, "workspace map");
constexpr unsigned L_Q = 0;
constexpr unsigned L_XN = 1024;
constexpr unsigned L_QP = L_XN + 4 * 448 * 4;
constexpr unsigned L_CNT = L_QP + 1024;
constexpr unsigned L_TM = L_CNT + 12;
constexpr unsigned L_MLW = L_CNT + 16;
constexpr unsigned L_FAC = L_MLW + 64;
constexpr unsigned L_FFN = L_FAC + 32;
constexpr unsigned L_FIN = L_FFN + 448 * 4;
constexpr unsigned L_CMB = L_FIN + 256;
constexpr unsigned L_P = L_CMB + 256;
constexpr unsigned L_ACC = L_P + 8192;
constexpr unsigned L_WO = L_ACC + 8192;
constexpr unsigned L_W2 = L_WO + 14336;
constexpr unsigned L_W1 = L_W2 + 7168;
constexpr unsigned L_TLB = L_W1 + 7168;
constexpr unsigned L_END = L_TLB + 128;
constexpr unsigned LDS_BYTES = 88 * 1024;
static_assert(L_END <= 65536 && L_P % 16 == 0 && L_FFN % 16 == 0 && L_FAC % 16 == 0 && L_MLW % 16 == 0 && L_WO % 16 == 0, "LDS map");

template <int V> struct IC { static constexpr int value = V; };

__device__ __forceinline__ float bflo(unsigned u) { return __builtin_bit_cast(float, u << 16); }
__device__ __forceinline__ float bfhi(unsigned u) { return __builtin_bit_cast(float, u & 0xffff0000u); }
__device__ __forceinline__ float rbf(float f) { return (float)(__bf16)f; }
__device__ __forceinline__ unsigned pk2(float lo, float hi) { bf16x2 v = {(__bf16)lo, (__bf16)hi}; return __builtin_bit_cast(unsigned, v); }
__device__ __forceinline__ float dot2(unsigned a, unsigned b, float c) {
  return __builtin_amdgcn_fdot2_f32_bf16(__builtin_bit_cast(bf16x2, a), __builtin_bit_cast(bf16x2, b), c, false);
}
template <int CTRL> __device__ __forceinline__ float dpp(float x) {
  return __builtin_bit_cast(float, __builtin_amdgcn_mov_dpp(__builtin_bit_cast(int, x), CTRL, 0xf, 0xf, true));
}
constexpr int XOR1 = 0xB1, XOR2 = 0x4E, XOR3 = 0x1B, XOR7 = 0x141, XOR8 = 0x128, ROR4 = 0x124;
__device__ __forceinline__ float xor4(float x) { return dpp<XOR3>(dpp<XOR7>(x)); }
__device__ __forceinline__ float sw16_sum(float a, float b) {
  auto r = __builtin_amdgcn_permlane16_swap(__float_as_uint(a), __float_as_uint(b), false, false);
  return __uint_as_float(r[0]) + __uint_as_float(r[1]);
}
__device__ __forceinline__ float sw32_sum(float a, float b) {
  auto r = __builtin_amdgcn_permlane32_swap(__float_as_uint(a), __float_as_uint(b), false, false);
  return __uint_as_float(r[0]) + __uint_as_float(r[1]);
}
__device__ __forceinline__ float sw16_max(float a) {
  auto r = __builtin_amdgcn_permlane16_swap(__float_as_uint(a), __float_as_uint(a), false, false);
  return fmaxf(__uint_as_float(r[0]), __uint_as_float(r[1]));
}
__device__ __forceinline__ float sw32_max(float a) {
  auto r = __builtin_amdgcn_permlane32_swap(__float_as_uint(a), __float_as_uint(a), false, false);
  return fmaxf(__uint_as_float(r[0]), __uint_as_float(r[1]));
}
template <int CTRL> __device__ __forceinline__ float tr_step(bool bit, float a, float b) { return (bit ? b : a) + dpp<CTRL>(bit ? a : b); }
__device__ __forceinline__ float reduce8_hi(float (&v)[8], int lane) {
#pragma unroll
  for (int k = 0; k < 4; ++k) v[k] = sw32_sum(v[k], v[k + 4]);
#pragma unroll
  for (int k = 0; k < 2; ++k) v[k] = sw16_sum(v[k], v[k + 2]);
  return tr_step<XOR8>(lane & 8, v[0], v[1]);
}
__device__ __forceinline__ float rdlane(float v, int l) { return __builtin_bit_cast(float, __builtin_amdgcn_readlane(__builtin_bit_cast(int, v), l)); }
__device__ __forceinline__ unsigned lds_ld(LAS unsigned* p) { return __hip_atomic_load(p, __ATOMIC_RELAXED, __HIP_MEMORY_SCOPE_WORKGROUP); }
__device__ __forceinline__ void lds_inc(LAS unsigned* p) { (void)__hip_atomic_fetch_add(p, 1u, __ATOMIC_RELAXED, __HIP_MEMORY_SCOPE_WORKGROUP); }
__device__ __forceinline__ bool tag_ok(const u32x4& v, unsigned tag) { return v[0] == tag && v[2] == tag; }
__device__ __forceinline__ u32x4 granule(unsigned tag, float a, float b) { return u32x4{tag, __float_as_uint(a), tag, __float_as_uint(b)}; }
__device__ __forceinline__ u32x4 ld_sc1(__amdgpu_buffer_rsrc_t r, unsigned off) { return __builtin_amdgcn_raw_buffer_load_b128(r, off, 0, 16); }
__device__ __forceinline__ void st_sc1(u32x4 v, __amdgpu_buffer_rsrc_t r, unsigned off) { __builtin_amdgcn_raw_buffer_store_b128(v, r, off, 0, 16); }
__device__ __forceinline__ void dma16(const void* sbase, unsigned voff, unsigned lds) {
  unsigned keep;
  asm volatile("s_mov_b32 %0, m0\n\ts_mov_b32 m0, %3\n\ts_nop 0\n\tglobal_load_lds_dwordx4 %1, %2 nt\n\ts_mov_b32 m0, %0" : "=&s"(keep) : "v"(voff), "s"(sbase), "s"(lds) : "memory");
}
#define BARRIER() do { __builtin_amdgcn_sched_barrier(0); asm volatile("s_waitcnt lgkmcnt(0)\n\ts_barrier" ::: "memory"); __builtin_amdgcn_sched_barrier(0); } while (0)
#define DRAIN() asm volatile("s_waitcnt vmcnt(0)" ::: "memory")
#define NOW() ((unsigned)__builtin_amdgcn_s_memrealtime())
__device__ __forceinline__ void pace(unsigned tgt) {
  asm volatile("pace_%=:\n\ts_memrealtime s[60:61]\n\ts_waitcnt lgkmcnt(0)\n\ts_sub_u32 s60, s60, %0\n\ts_cmp_lt_i32 s60, 0\n\ts_cbranch_scc1 pace_%=" :: "s"(tgt) : "memory", "scc", "s60", "s61");
}

struct Args {
  const unsigned short *x, *kc, *vc, *wq, *wo, *w1, *w2;
  unsigned short* out;
  unsigned char* ws;
};

__global__ void __launch_bounds__(NTHR, 2) fused_decode(Args a) {
  extern __shared__ __attribute__((aligned(16))) unsigned char smem[];
  LAS unsigned char* L = (LAS unsigned char*)smem;
  const int tid = threadIdx.x, lane = tid & 63, w = __builtin_amdgcn_readfirstlane(tid >> 6);
  const int b = blockIdx.x, g = b & 7, s = b >> 3;

  if (w < 4) {
    const int hf = lane >> 5, c = lane & 31;
    const unsigned voff = lane * 16;
    const unsigned sbase = (unsigned)s * (1024u * 512u) + (unsigned)w * 2048u;
    const unsigned short* kh = a.kc + (size_t)g * SK * DH;
    const unsigned short* vh = a.vc + (size_t)g * SK * DH;
    auto soff = [&](int p) -> unsigned { return sbase + ((unsigned)((p + s) & 63) << 13); };
    u32x4 buf[W];
    BARRIER();
    auto dma_weights = [&]() {
      const unsigned short* wos = a.wo + (size_t)(g * 512 + s * 16) * D;
#pragma unroll
      for (int i = 0; i < 4; ++i) if (w * 4 + i < 14) dma16(wos, voff + (w * 4 + i) * 1024, L_WO + (w * 4 + i) * 1024);
      if (s < 28) {
        const int jf = g * 28 + s;
        const unsigned short* w2s = a.w2 + (size_t)jf * 8 * D;
        const unsigned short* w1s = a.w1 + (size_t)(jf & 7) * 56 * DFF + (jf >> 3) * 64;
#pragma unroll
        for (int i = 0; i < 2; ++i) if (w * 2 + i < 7) {
          dma16(w2s, voff + (w * 2 + i) * 1024, L_W2 + (w * 2 + i) * 1024);
          dma16(w1s, (unsigned)((w * 2 + i) * 8 + (lane >> 3)) * (DFF * 2) + (lane & 7) * 16, L_W1 + (w * 2 + i) * 1024);
        }
      }
    };
    const __amdgpu_buffer_rsrc_t rk = __builtin_amdgcn_make_buffer_rsrc((void*)kh, 0, HEADB, 0x00020000);
#pragma unroll
    for (int i = 0; i < 16; ++i) {
      if (i >= P0) asm volatile("s_waitcnt vmcnt(%0)" ::"i"(P0 - 1) : "memory");
      buf[i] = __builtin_amdgcn_raw_buffer_load_b128(rk, voff + (i & 1) * 1024, soff(i >> 1), 2);
      __builtin_amdgcn_sched_barrier(0);
    }
    DRAIN();
    dma_weights();
    BARRIER();
    const unsigned tb = __builtin_amdgcn_readfirstlane(*(const LAS unsigned*)(L + L_TM)) + (unsigned)TOFF;
#pragma unroll
    for (int i = 16; i < W; ++i) {
      if ((i & 3) == 0) pace(tb + (((i - 16) * TAU) >> 8));
      buf[i] = __builtin_amdgcn_raw_buffer_load_b128(rk, voff + (i & 1) * 1024, soff(i >> 1), 2);
      __builtin_amdgcn_sched_barrier(0);
    }
    const u32x4 q0 = *(const LAS u32x4*)(L + L_Q + c * 16), q1 = *(const LAS u32x4*)(L + L_Q + 512 + c * 16);
    LAS float* pl = (LAS float*)(L + L_P) + w * 512 + hf * 256;

    float m = -INFINITY;
    auto k_group = [&](auto slot, int j) {
      constexpr int S0 = decltype(slot)::value * 8;
      const int jn = j + RG;
      const __amdgpu_buffer_rsrc_t rn = __builtin_amdgcn_make_buffer_rsrc((void*)(jn < 16 ? kh : vh), 0, HEADB, 0x00020000);
      const int pb = (jn & 15) * 4;
      const unsigned tg = tb + (((unsigned)(j * 8 + W - 16) * TAU) >> 8);
      float x[16];
#pragma unroll
      for (int i = 0; i < 8; ++i) {
        float s0 = 0.f, s1 = 0.f;
#pragma unroll
        for (int k = 0; k < 4; ++k) { s0 = dot2(buf[S0 + i][k], q0[k], s0); s1 = dot2(buf[S0 + i][k], q1[k], s1); }
        x[2 * i] = s0; x[2 * i + 1] = s1;
        asm volatile("" : "+v"(x[2 * i]), "+v"(x[2 * i + 1]) :: "memory");
        if ((i & 3) == 0) pace(tg + ((i * TAU) >> 8));
        buf[S0 + i] = __builtin_amdgcn_raw_buffer_load_b128(rn, voff + (i & 1) * 1024, soff(pb + (i >> 1)), 2);
        __builtin_amdgcn_sched_barrier(0);
      }
#pragma unroll
      for (int h = 0; h < 8; ++h) x[h] = sw16_sum(x[h], x[h + 8]);
#pragma unroll
      for (int h = 0; h < 4; ++h) x[h] = tr_step<XOR8>(c & 8, x[h], x[h + 4]);
#pragma unroll
      for (int h = 0; h < 2; ++h) x[h] = tr_step<XOR7>(c & 4, x[h], x[h + 2]);
      x[0] = tr_step<XOR2>(c & 2, x[0], x[1]);
      x[0] += dpp<XOR1>(x[0]);
      const float sv = rbf(x[0]) * 0.0625f;
      pl[j * 16 + (c >> 1)] = sv;
      m = fmaxf(m, sv);
      __builtin_amdgcn_sched_barrier(0);
    };
#pragma unroll 1
    for (int j = 0; j < (16 / RG) * RG; j += RG) {
      k_group(IC<0>{}, j); k_group(IC<1>{}, j + 1);
      if constexpr (RG > 2) k_group(IC<2 % RG>{}, j + 2);
      if constexpr (RG > 3) k_group(IC<3 % RG>{}, j + 3);
      if (j == 0 && lane == 0) lds_inc((LAS unsigned*)(L + L_CNT) + 2);
    }
    if constexpr (16 % RG == 1) k_group(IC<0>{}, 15);

    float l = 0.f;
    {
      float sc[8];
#pragma unroll
      for (int k = 0; k < 8; ++k) sc[k] = pl[k * 32 + c];
      m = fmaxf(m, dpp<ROR4>(m)); m = fmaxf(m, dpp<XOR8>(m)); m = sw16_max(m); m = sw32_max(m);
      m = dpp<0x88>(m);
      const float ml2 = m * 1.4426950408889634f;
#pragma unroll
      for (int k = 0; k < 8; ++k) { const float p = __builtin_amdgcn_exp2f(fmaf(sc[k], 1.4426950408889634f, -ml2)); l += p; pl[k * 32 + c] = p; }
    }

    f32x2 ac2[8];
#pragma unroll
    for (int k = 0; k < 8; ++k) ac2[k] = f32x2{0.f, 0.f};
    auto v_group = [&](auto slot, int j) {
      constexpr int S0 = decltype(slot)::value * 8;
      const int jn = j + RG;
      const bool more = jn < 32;
      const __amdgpu_buffer_rsrc_t rn = __builtin_amdgcn_make_buffer_rsrc((void*)vh, 0, more ? HEADB : 0, 0x00020000);
      const int pb = (jn & 15) * 4;
      const unsigned tg = tb + ((TGAP + (((unsigned)(j * 8 + W - 16) * TAU) >> 8)) & (more ? ~0u : 0u));
      const LAS float* pj = pl + (j - 16) * 16;
      f32x4 pp = *(const LAS f32x4*)pj;
#pragma unroll
      for (int i = 0; i < 8; i += 2) {
        const f32x4 pc = pp;
        if (i + 2 < 8) pp = *(const LAS f32x4*)(pj + (i + 2) * 2);
#pragma unroll
        for (int u = 0; u < 2; ++u) {
          const f32x2 pa = {pc[2 * u], pc[2 * u]}, pb2 = {pc[2 * u + 1], pc[2 * u + 1]};
#pragma unroll
          for (int k = 0; k < 4; ++k) {
            const f32x2 vv = {bflo(buf[S0 + i + u][k]), bfhi(buf[S0 + i + u][k])};
            ac2[k] = __builtin_elementwise_fma(pa, vv, ac2[k]);
            ac2[4 + k] = __builtin_elementwise_fma(pb2, vv, ac2[4 + k]);
          }
          asm volatile("" : "+v"(ac2[0]), "+v"(ac2[1]), "+v"(ac2[2]), "+v"(ac2[3]), "+v"(ac2[4]), "+v"(ac2[5]), "+v"(ac2[6]), "+v"(ac2[7]) :: "memory");
          if (((i + u) & 3) == 0) pace(tg + (((i + u) * TAU) >> 8));
          buf[S0 + i + u] = __builtin_amdgcn_raw_buffer_load_b128(rn, voff + u * 1024, soff(pb + (i >> 1)), 2);
          __builtin_amdgcn_sched_barrier(0);
        }
      }
    };
    constexpr int V0 = 16 % RG;
#pragma unroll 1
    for (int j = 16; j < 16 + (16 / RG) * RG; j += RG) {
      v_group(IC<V0>{}, j); v_group(IC<(V0 + 1) % RG>{}, j + 1);
      if constexpr (RG > 2) v_group(IC<(V0 + 2) % RG>{}, j + 2);
      if constexpr (RG > 3) v_group(IC<(V0 + 3) % RG>{}, j + 3);
      if (j == 16) {
        l += dpp<XOR2>(l); l += dpp<ROR4>(l); l += dpp<XOR8>(l); l = sw16_sum(l, l); l = sw32_sum(l, l);
        if (lane < 2) *(LAS f32x2*)(L + L_MLW + (w * 2 + lane) * 8) = f32x2{m, l};
        if (lane == 0) lds_inc((LAS unsigned*)(L + L_CNT) + 1);
      }
    }
    if constexpr (16 % RG == 1) v_group(IC<31 % RG>{}, 31);
    {
      float acc[8];
#pragma unroll
      for (int k = 0; k < 4; ++k) { acc[2 * k] = sw32_sum(ac2[k][0], ac2[4 + k][0]); acc[2 * k + 1] = sw32_sum(ac2[k][1], ac2[4 + k][1]); }
      LAS float* rec = (LAS float*)(L + L_ACC) + (w * 2 + hf) * 256 + 8 * c;
      *(LAS f32x4*)rec = f32x4{acc[0], acc[1], acc[2], acc[3]};
      *(LAS f32x4*)(rec + 4) = f32x4{acc[4], acc[5], acc[6], acc[7]};
    }
    BARRIER();
    return;
  }

  const unsigned t0h = NOW();
  const int hw = w - 4, ht = tid - 256;
  const __amdgpu_buffer_rsrc_t rws = __builtin_amdgcn_make_buffer_rsrc((void*)a.ws, 0, WS_USED, 0x00020000);
  const __amdgpu_buffer_rsrc_t rx = __builtin_amdgcn_make_buffer_rsrc((void*)a.x, 0, D * 2, 0x00020000);
  LAS unsigned* cnt = (LAS unsigned*)(L + L_CNT);
  LAS float* xn = (LAS float*)(L + L_XN) + hw * 448;
  unsigned tag;
  {
    const int cb = s >> 2, kq = s & 3;
    const __amdgpu_buffer_rsrc_t rq = __builtin_amdgcn_make_buffer_rsrc((void*)(a.wq + (size_t)kq * 112 * NQ), 0, 112 * NQ * 2, 0x00020000);
    const u32x4 xv = __builtin_amdgcn_raw_buffer_load_b128(rx, lane * 16, 0, 0);
    const int cg = ht & 7, rs = ht >> 3;
    u32x4 wv[4];
    const unsigned qo = (unsigned)rs * (NQ * 2) + (unsigned)(g * 512 + cb * 64 + cg * 8) * 2;
#pragma unroll
    for (int i = 0; i < 4; ++i) wv[i] = __builtin_amdgcn_raw_buffer_load_b128(rq, qo + i * 32 * (NQ * 2), 0, 2);
    const unsigned ep = __builtin_amdgcn_raw_buffer_load_b32(rws, 0, WS_EPOCH, 16);
    if (ht < 3) cnt[ht] = 0u;
    BARRIER();
    if (hw == 3 && lane < 8)
      dma16(a.vc + ((size_t)g * SK + (size_t)s * 1024) * DH, lane * 16, L_TLB);
    float f[8];
#pragma unroll
    for (int i = 0; i < 4; ++i) { f[2 * i] = bflo(xv[i]); f[2 * i + 1] = bfhi(xv[i]); }
    const float x0 = rdlane(f[0], 0);
    float s1 = 0.f, s2 = 0.f;
#pragma unroll
    for (int i = 0; i < 8; ++i) { const float d = f[i] - x0; s1 += d; s2 = fmaf(d, d, s2); }
    if (lane >= 56) { s1 = 0.f; s2 = 0.f; }
    s1 += dpp<XOR1>(s1); s2 += dpp<XOR1>(s2); s1 += dpp<XOR2>(s1); s2 += dpp<XOR2>(s2);
    s1 += dpp<XOR7>(s1); s2 += dpp<XOR7>(s2); s1 += dpp<XOR8>(s1); s2 += dpp<XOR8>(s2);
    { const float t = sw16_sum(s1, s2);
      const float t2 = sw32_sum(t, t); s1 = rdlane(t2, 0); s2 = rdlane(t2, 16); }
    const float md = s1 * (1.0f / D);
    const float var = fmaxf(fmaf(-md, md, s2 * (1.0f / D)), 0.f);
    const float mb = rbf(x0 + md), den = rbf(sqrtf(rbf(rbf(var) + EPS)));
    const float rden = 1.0f / den;
    if (lane < 56) {
      f32x4 n0, n1;
#pragma unroll
      for (int i = 0; i < 4; ++i) { n0[i] = rbf(rbf(f[i] - mb) * rden); n1[i] = rbf(rbf(f[4 + i] - mb) * rden); }
      *(LAS f32x4*)(xn + lane * 8) = n0; *(LAS f32x4*)(xn + lane * 8 + 4) = n1;
    }
    asm volatile("" ::: "memory");
    float xr[4];
#pragma unroll
    for (int i = 0; i < 4; ++i) { const int row = rs + 32 * i; xr[i] = xn[kq * 112 + (row < 112 ? row : 0)]; }
    float qa[8];
#pragma unroll
    for (int k = 0; k < 8; ++k) qa[k] = 0.f;
#pragma unroll
    for (int i = 0; i < 4; ++i) {
#pragma unroll
      for (int k = 0; k < 4; ++k) { qa[2 * k] = fmaf(xr[i], bflo(wv[i][k]), qa[2 * k]); qa[2 * k + 1] = fmaf(xr[i], bfhi(wv[i][k]), qa[2 * k + 1]); }
    }
    LAS float* qp = (LAS float*)(L + L_QP);
    qp[hw * 64 + cg * 8 + (lane >> 3)] = reduce8_hi(qa, lane);
    if (lane == 0) lds_inc(cnt);
    tag = __builtin_amdgcn_readfirstlane(ep) + 1u;
    st_sc1(u32x4{tag, t0h, tag, t0h}, rws, s == 0 && ht == 0 ? WS_XT + g * 128 : OOB);
    for (unsigned spin = 0; spin < SPIN_CAP; ++spin) { if (__builtin_amdgcn_readfirstlane(lds_ld(cnt)) == 4u) break; }
    asm volatile("" ::: "memory");
    const int pr = ht & 31, cbt = ht >> 5;
    f32x2 own = *(const LAS f32x2*)(qp + 2 * pr);
#pragma unroll
    for (int k = 1; k < 4; ++k) own += *(const LAS f32x2*)(qp + k * 64 + 2 * pr);
    st_sc1(granule(tag, own[0], own[1]), rws, (lane >> 3) == hw ? WS_XQ + ((g * 8 + cb) * 4 + kq) * 512 + pr * 16 : OOB);
    DRAIN();
    const u32x4 gt = ld_sc1(rws, hw == 0 && lane < 8 ? WS_XT + lane * 128 : OOB);
    u32x4 gq[4]; bool ok[4];
#pragma unroll
    for (int k = 0; k < 4; ++k) { ok[k] = cbt == cb && k == kq; gq[k] = granule(tag, own[0], own[1]); }
    for (unsigned spin = 0; spin < SPIN_CAP; ++spin) {
      asm volatile("" ::: "memory");
#pragma unroll
      for (int k = 0; k < 4; ++k) if (!ok[k]) gq[k] = ld_sc1(rws, WS_XQ + ((g * 8 + cbt) * 4 + k) * 512 + pr * 16);
#pragma unroll
      for (int k = 0; k < 4; ++k) ok[k] = tag_ok(gq[k], tag);
      if (__all(ok[0] && ok[1] && ok[2] && ok[3])) break;
      __builtin_amdgcn_s_sleep(2);
    }
    { const float q0 = (__uint_as_float(gq[0][1]) + __uint_as_float(gq[1][1])) + (__uint_as_float(gq[2][1]) + __uint_as_float(gq[3][1]));
      const float q1 = (__uint_as_float(gq[0][3]) + __uint_as_float(gq[1][3])) + (__uint_as_float(gq[2][3]) + __uint_as_float(gq[3][3]));
      *(LAS unsigned*)(L + L_Q + ht * 4) = pk2(q0, q1); }
    if (hw == 0) {
      int dm = tag_ok(gt, tag) ? min(0, (int)(gt[1] - t0h)) : 0;
      dm = min(dm, __builtin_bit_cast(int, dpp<XOR1>(__builtin_bit_cast(float, dm))));
      dm = min(dm, __builtin_bit_cast(int, dpp<XOR2>(__builtin_bit_cast(float, dm))));
      dm = min(dm, __builtin_bit_cast(int, dpp<XOR7>(__builtin_bit_cast(float, dm))));
      if (lane == 0) *(LAS unsigned*)(L + L_TM) = t0h + (unsigned)max(dm, -300);
    }
  }
  BARRIER();
  for (unsigned spin = 0; spin < SPIN_CAP; ++spin) { if (__builtin_amdgcn_readfirstlane(lds_ld(cnt + 2)) == 4u) break; __builtin_amdgcn_s_sleep(4); }
  asm volatile("" ::: "memory");
  f32x2 wo2[16];
#pragma unroll
  for (int r = 0; r < 16; ++r) {
    const unsigned v = *(const LAS unsigned*)(L + L_WO + r * (D * 2) + (ht < 224 ? ht : 0) * 4);
    wo2[r] = f32x2{bflo(v), bfhi(v)}; asm volatile("" : "+v"(wo2[r]));
  }
  unsigned xres = __builtin_amdgcn_raw_buffer_load_b32(rx, b < 224 / GP && ht < GP ? (b * GP + ht) * 4 : OOB, 0, 0);
  if (hw == 1) {
    const bool has_ffn = s < 28;
    const int j = g * 28 + s, cbh = j >> 3, e = j & 7, cg = lane & 7, rs = lane >> 3;
    float hv0 = 0.f, hv1 = 0.f;
    if (has_ffn) {
      float ha[8];
#pragma unroll
      for (int k = 0; k < 8; ++k) ha[k] = 0.f;
#pragma unroll
      for (int i = 0; i < 7; ++i) {
        const u32x4 wv = *(const LAS u32x4*)(L + L_W1 + i * 1024 + lane * 16);
        const float xr = xn[e * 56 + rs + 8 * i];
#pragma unroll
        for (int k = 0; k < 4; ++k) { ha[2 * k] = fmaf(xr, bflo(wv[k]), ha[2 * k]); ha[2 * k + 1] = fmaf(xr, bfhi(wv[k]), ha[2 * k + 1]); }
      }
      const float hs = reduce8_hi(ha, lane), hn = dpp<XOR8>(hs);
      st_sc1(granule(tag, hs, hn), rws, (lane & 8) == 0 ? WS_XH + j * 512 + (cg * 4 + (lane >> 4)) * 16 : OOB);
      DRAIN();
      pace(t0h + FFG);
      u32x4 gh = {0, 0, 0, 0}; bool ok = lane >= 32;
      for (unsigned spin = 0; spin < SPIN_CAP; ++spin) {
        asm volatile("" ::: "memory");
        if (!ok) { gh = ld_sc1(rws, WS_XH + (cbh * 8 + (lane >> 2)) * 512 + (e * 4 + (lane & 3)) * 16); ok = tag_ok(gh, tag); }
        if (__all(ok)) break;
        __builtin_amdgcn_s_sleep(16);
      }
      hv0 = lane < 32 ? __uint_as_float(gh[1]) : 0.f; hv1 = lane < 32 ? __uint_as_float(gh[3]) : 0.f;
      hv0 += xor4(hv0); hv1 += xor4(hv1); hv0 += dpp<XOR8>(hv0); hv1 += dpp<XOR8>(hv1);
      hv0 = sw16_sum(hv0, hv0); hv1 = sw16_sum(hv1, hv1);
      hv0 = fmaxf(rbf(hv0), 0.f); hv1 = fmaxf(rbf(hv1), 0.f);
    }
    float fo[8];
#pragma unroll
    for (int k = 0; k < 8; ++k) fo[k] = 0.f;
    if (has_ffn) {
#pragma unroll
      for (int r = 0; r < 8; ++r) {
        const u32x4 wv = *(const LAS u32x4*)(L + L_W2 + r * (D * 2) + (lane < 56 ? lane : 0) * 16);
        const float hr = rdlane((r & 1) ? hv1 : hv0, r >> 1);
#pragma unroll
        for (int k = 0; k < 4; ++k) { fo[2 * k] = fmaf(hr, bflo(wv[k]), fo[2 * k]); fo[2 * k + 1] = fmaf(hr, bfhi(wv[k]), fo[2 * k + 1]); }
      }
    }
    if (lane < 56) { LAS float* ff = (LAS float*)(L + L_FFN) + lane * 8;
      *(LAS f32x4*)ff = f32x4{fo[0], fo[1], fo[2], fo[3]}; *(LAS f32x4*)(ff + 4) = f32x4{fo[4], fo[5], fo[6], fo[7]}; }
  }
  if (hw == 0) {
    for (unsigned spin = 0; spin < SPIN_CAP; ++spin) { if (__builtin_amdgcn_readfirstlane(lds_ld(cnt + 1)) == 4u) break; __builtin_amdgcn_s_sleep(8); }
    asm volatile("" ::: "memory");
    const int h = lane & 1, ww = (lane >> 1) & 3;
    const f32x2 ml = *(const LAS f32x2*)(L + L_MLW + (ww * 2 + h) * 8);
    float M = fmaxf(ml[0], dpp<XOR2>(ml[0])); M = fmaxf(M, xor4(M));
    float Ls = __expf(ml[0] - M) * ml[1]; Ls += dpp<XOR2>(Ls); Ls += xor4(Ls);
    st_sc1(granule(tag, M, Ls), rws, lane < 2 ? WS_ML + (g * 32 + s) * 32 + lane * 16 : OOB);
    DRAIN();
    pace(t0h + MLG);
    u32x4 gm = {0, 0, 0, 0}; bool ok = false;
    for (unsigned spin = 0; spin < SPIN_CAP; ++spin) {
      asm volatile("" ::: "memory");
      if (!ok) gm = ld_sc1(rws, WS_ML + g * 1024 + lane * 16);
      ok = tag_ok(gm, tag);
      if (__all(ok)) break;
      __builtin_amdgcn_s_sleep(16);
    }
    const float ms = __uint_as_float(gm[1]);
    float Mg = fmaxf(ms, dpp<XOR2>(ms)); Mg = fmaxf(Mg, xor4(Mg)); Mg = fmaxf(Mg, dpp<XOR8>(Mg)); Mg = sw16_max(Mg); Mg = sw32_max(Mg);
    float Lg = __expf(ms - Mg) * __uint_as_float(gm[3]);
    Lg += dpp<XOR2>(Lg); Lg += xor4(Lg); Lg += dpp<XOR8>(Lg); Lg = sw16_sum(Lg, Lg); Lg = sw32_sum(Lg, Lg);
    if (lane < 8) ((LAS float*)(L + L_FAC))[h * 4 + ww] = __expf(ml[0] - Mg) / Lg;
  }
  unsigned off_x1 = WS_X1 + (g * 32 + s) * 4096 + ht * 16;
  unsigned off_g1 = WS_X1 + (g * 32 + (ht >> 3)) * 4096 + s * 128 + (ht & 7) * 16;
  unsigned off_x2 = ht < 224 ? WS_X2 + (((unsigned)ht / GP) * 256 + b) * (GP * 16) + ((unsigned)ht % GP) * 16 : OOB;
  unsigned off_g2 = WS_X2 + b * (GP * 4096) + ht * 16;
  unsigned lds_acc = L_ACC + (ht >> 7) * 1024 + (ht & 127) * 8, lds_fac = L_FAC + (ht >> 7) * 16, lds_ffn = L_FFN + (ht < 224 ? ht : 0) * 8;
  unsigned lds_cmw = L_CMB + hw * 64 + (lane & 7) * 8 + (lane >> 4) * 4, lds_cmr = L_CMB + (lane & 15) * 4;
  asm volatile("" : "+v"(xres), "+v"(off_x1), "+v"(off_g1), "+v"(off_x2), "+v"(off_g2), "+v"(lds_acc), "+v"(lds_fac), "+v"(lds_ffn), "+v"(lds_cmw), "+v"(lds_cmr));
  BARRIER();
  f32x2 o2;
  {
    const f32x4 fac = *(const LAS f32x4*)(L + lds_fac);
    const LAS float* ar = (const LAS float*)(L + lds_acc);
    const f32x2 O = __builtin_elementwise_fma(f32x2{fac[0], fac[0]}, *(const LAS f32x2*)ar, f32x2{fac[1], fac[1]} * *(const LAS f32x2*)(ar + 512)) +
                    __builtin_elementwise_fma(f32x2{fac[2], fac[2]}, *(const LAS f32x2*)(ar + 1024), f32x2{fac[3], fac[3]} * *(const LAS f32x2*)(ar + 1536));
    st_sc1(granule(tag, O[0], O[1]), rws, off_x1);
    o2 = *(const LAS f32x2*)(L + lds_ffn);
  }
  __builtin_amdgcn_s_sleep(DELAY1);
  {
    u32x4 go = {0, 0, 0, 0}; bool ok = false;
    for (unsigned spin = 0; spin < SPIN_CAP; ++spin) {
      asm volatile("" ::: "memory");
      if (!ok) go = ld_sc1(rws, off_g1);
      ok = tag_ok(go, tag);
      if (__all(ok)) break;
      __builtin_amdgcn_s_sleep(1);
    }
    float a0 = __uint_as_float(go[1]), a1 = __uint_as_float(go[3]);
    a0 += dpp<XOR8>(a0); a1 += dpp<XOR8>(a1);
    const float t = sw16_sum(a0, a1);
    const float t2 = sw32_sum(t, t);
    if ((lane & 0x28) == 0) *(LAS float*)(L + lds_cmw) = t2;
    BARRIER();
    const LAS float* cm = (const LAS float*)(L + lds_cmr);
    const float ov = rbf((cm[0] + cm[16]) + (cm[32] + cm[48]));
    f32x2 o3 = {0.f, 0.f};
#pragma unroll
    for (int r = 0; r < 8; ++r) {
      const float oa = rdlane(ov, r), ob = rdlane(ov, 8 + r);
      o2 = __builtin_elementwise_fma(f32x2{oa, oa}, wo2[r], o2); o3 = __builtin_elementwise_fma(f32x2{ob, ob}, wo2[8 + r], o3);
    }
    o2 += o3;
    st_sc1(granule(tag, o2[0], o2[1]), rws, off_x2);
  }
  if (b < 224 / GP) {
    __builtin_amdgcn_s_sleep(DELAY2);
    u32x4 gv[GP]; bool ok[GP];
#pragma unroll
    for (int k = 0; k < GP; ++k) { gv[k] = u32x4{0, 0, 0, 0}; ok[k] = false; }
    for (unsigned spin = 0; spin < SPIN_CAP; ++spin) {
      asm volatile("" ::: "memory");
      bool all = true;
#pragma unroll
      for (int k = 0; k < GP; ++k) if (!ok[k]) gv[k] = __builtin_amdgcn_raw_buffer_load_b128(rws, off_g2, k * 4096, 16);
#pragma unroll
      for (int k = 0; k < GP; ++k) { ok[k] = tag_ok(gv[k], tag); all = all && ok[k]; }
      if (__all(all)) break;
    }
    float a0 = 0.f, a1 = 0.f;
#pragma unroll
    for (int k = 0; k < GP; ++k) { a0 += __uint_as_float(gv[k][1]); a1 += __uint_as_float(gv[k][3]); }
    if (GP < 2) { a0 += dpp<XOR1>(a0); a1 += dpp<XOR1>(a1); }
    if (GP < 4) { a0 += dpp<XOR2>(a0); a1 += dpp<XOR2>(a1); }
    if (GP < 8) { a0 += xor4(a0); a1 += xor4(a1); }
    a0 += dpp<XOR8>(a0); a1 += dpp<XOR8>(a1);
    const float t = sw16_sum(a0, a1), t2 = sw32_sum(t, t);
    LAS float* fin = (LAS float*)(L + L_FIN);
    if ((lane & 0x2f) < GP) fin[hw * 16 + 2 * (lane & 7) + (lane >> 4)] = t2;
    BARRIER();
    if (ht < GP) {
      const LAS f32x2* f2 = (const LAS f32x2*)fin + ht;
      const f32x2 r = f32x2{bflo(xres), bfhi(xres)} + ((f2[0] + f2[8]) + (f2[16] + f2[24]));
      ((GAS unsigned*)a.out)[b * GP + ht] = pk2(r[0], r[1]);
    }
    __builtin_amdgcn_raw_buffer_store_b32(tag, rws, b == 0 && ht == 0 ? WS_EPOCH : OOB, 0, 16);
  }
}

extern "C" void kernel_launch(void* const* d_in, const int* in_sizes, int n_in, void* d_out, int out_size, void* d_ws, size_t ws_size, hipStream_t stream) {
  Args a;
  a.x = (const unsigned short*)d_in[0]; a.kc = (const unsigned short*)d_in[1]; a.vc = (const unsigned short*)d_in[2];
  a.wq = (const unsigned short*)d_in[3]; a.wo = (const unsigned short*)d_in[4]; a.w1 = (const unsigned short*)d_in[6]; a.w2 = (const unsigned short*)d_in[7];
  a.out = (unsigned short*)d_out; a.ws = (unsigned char*)d_ws;
  (void)hipFuncSetAttribute((const void*)fused_decode, hipFuncAttributeMaxDynamicSharedMemorySize, LDS_BYTES);
  hipLaunchKernelGGL(fused_decode, dim3(NWG), dim3(NTHR), LDS_BYTES, stream, a);
}
```

```cpp
#include <hip/hip_runtime.h>
#include <stdint.h>

typedef unsigned u32x4 __attribute__((ext_vector_type(4)));
typedef float f32x4 __attribute__((ext_vector_type(4)));
typedef float f32x2 __attribute__((ext_vector_type(2)));
typedef __bf16 bf16x2 __attribute__((ext_vector_type(2)));
#define LAS __attribute__((address_space(3)))
#define GAS __attribute__((address_space(1)))

constexpr int RG = 2;
constexpr int P0 = 5;
constexpr unsigned TAU = 3710;
constexpr unsigned TOFF = 304;
constexpr unsigned TGAP = 25;
constexpr int DELAY1 = 12, DELAY2 = 13;
constexpr unsigned MLG = 3000;
constexpr unsigned FFG = 1200;
constexpr int GP = 4;

constexpr int D = 448, SK = 32768, DH = 256, DFF = 1792, NQ = 4096;
constexpr int NWG = 256, NTHR = 512, W = RG * 8;
constexpr int HEADB = SK * DH * 2;
constexpr float EPS = 1e-5f, LOG2E = 1.4426950408889634f;
constexpr unsigned SPIN_CAP = 1u << 18;
constexpr unsigned OOB = 0x7ffffff0u;
static_assert(P0 < 16 && RG >= 2 && RG <= 4, "ring");
constexpr unsigned WS_EPOCH = 0;
constexpr unsigned WS_ML = 4096;
constexpr unsigned WS_XT = 12288;
constexpr unsigned WS_XQ = 16384;
constexpr unsigned WS_XH = WS_XQ + 131072;
constexpr unsigned WS_X1 = 524288;
constexpr unsigned WS_X2 = WS_X1 + (1u << 20);
constexpr unsigned WS_USED = WS_X2 + 56 * 16384;
static_assert(WS_ML + 8192 <= WS_XT && WS_XT + 1024 <= WS_XQ && WS_XH + 224 * 512 <= WS_X1, "workspace map");
constexpr unsigned L_Q = 0;
constexpr unsigned L_XN = 1024;
constexpr unsigned L_QP = L_XN + 4 * 448 * 4;
constexpr unsigned L_CNT = L_QP + 1024;
constexpr unsigned L_TM = L_CNT + 12;
constexpr unsigned L_MLW = L_CNT + 16;
constexpr unsigned L_FAC = L_MLW + 64;
constexpr unsigned L_FFN = L_FAC + 32;
constexpr unsigned L_FIN = L_FFN + 448 * 4;
constexpr unsigned L_CMB = L_FIN + 256;
constexpr unsigned L_P = L_CMB + 256;
constexpr unsigned L_ACC = L_P + 8192;
constexpr unsigned L_WO = L_ACC + 8192;
constexpr unsigned L_W2 = L_WO + 14336;
constexpr unsigned L_W1 = L_W2 + 7168;
constexpr unsigned L_TLB = L_W1 + 7168;
constexpr unsigned L_END = L_TLB + 128;
constexpr unsigned LDS_BYTES = 88 * 1024;
static_assert(L_END <= 65536 && L_P % 16 == 0 && L_FFN % 16 == 0 && L_FAC % 16 == 0 && L_MLW % 16 == 0 && L_WO % 16 == 0, "LDS map");

template <int V> struct IC { static constexpr int value = V; };

__device__ __forceinline__ float bflo(unsigned u) { return __builtin_bit_cast(float, u << 16); }
__device__ __forceinline__ float bfhi(unsigned u) { return __builtin_bit_cast(float, u & 0xffff0000u); }
__device__ __forceinline__ float rbf(float f) { return (float)(__bf16)f; }
__device__ __forceinline__ unsigned pk2(float lo, float hi) { bf16x2 v = {(__bf16)lo, (__bf16)hi}; return __builtin_bit_cast(unsigned, v); }
__device__ __forceinline__ float dot2(unsigned a, unsigned b, float c) {
  return __builtin_amdgcn_fdot2_f32_bf16(__builtin_bit_cast(bf16x2, a), __builtin_bit_cast(bf16x2, b), c, false);
}
template <int CTRL> __device__ __forceinline__ float dpp(float x) {
  return __builtin_bit_cast(float, __builtin_amdgcn_mov_dpp(__builtin_bit_cast(int, x), CTRL, 0xf, 0xf, true));
}
constexpr int XOR1 = 0xB1, XOR2 = 0x4E, XOR3 = 0x1B, XOR7 = 0x141, XOR8 = 0x128, ROR4 = 0x124;
__device__ __forceinline__ float xor4(float x) { return dpp<XOR3>(dpp<XOR7>(x)); }
__device__ __forceinline__ float sw16_sum(float a, float b) {
  auto r = __builtin_amdgcn_permlane16_swap(__float_as_uint(a), __float_as_uint(b), false, false);
  return __uint_as_float(r[0]) + __uint_as_float(r[1]);
}
__device__ __forceinline__ float sw32_sum(float a, float b) {
  auto r = __builtin_amdgcn_permlane32_swap(__float_as_uint(a), __float_as_uint(b), false, false);
  return __uint_as_float(r[0]) + __uint_as_float(r[1]);
}
__device__ __forceinline__ float sw16_max(float a) {
  auto r = __builtin_amdgcn_permlane16_swap(__float_as_uint(a), __float_as_uint(a), false, false);
  return fmaxf(__uint_as_float(r[0]), __uint_as_float(r[1]));
}
__device__ __forceinline__ float sw32_max(float a) {
  auto r = __builtin_amdgcn_permlane32_swap(__float_as_uint(a), __float_as_uint(a), false, false);
  return fmaxf(__uint_as_float(r[0]), __uint_as_float(r[1]));
}
template <int CTRL> __device__ __forceinline__ float tr_step(bool bit, float a, float b) { return (bit ? b : a) + dpp<CTRL>(bit ? a : b); }
__device__ __forceinline__ float reduce8_hi(float (&v)[8], int lane) {
#pragma unroll
  for (int k = 0; k < 4; ++k) v[k] = sw32_sum(v[k], v[k + 4]);
#pragma unroll
  for (int k = 0; k < 2; ++k) v[k] = sw16_sum(v[k], v[k + 2]);
  return tr_step<XOR8>(lane & 8, v[0], v[1]);
}
__device__ __forceinline__ float rdlane(float v, int l) { return __builtin_bit_cast(float, __builtin_amdgcn_readlane(__builtin_bit_cast(int, v), l)); }
__device__ __forceinline__ unsigned lds_ld(LAS unsigned* p) { return __hip_atomic_load(p, __ATOMIC_RELAXED, __HIP_MEMORY_SCOPE_WORKGROUP); }
__device__ __forceinline__ void lds_inc(LAS unsigned* p) { (void)__hip_atomic_fetch_add(p, 1u, __ATOMIC_RELAXED, __HIP_MEMORY_SCOPE_WORKGROUP); }
__device__ __forceinline__ bool tag_ok(const u32x4& v, unsigned tag) { return v[0] == tag && v[2] == tag; }
__device__ __forceinline__ u32x4 granule(unsigned tag, float a, float b) { return u32x4{tag, __float_as_uint(a), tag, __float_as_uint(b)}; }
__device__ __forceinline__ u32x4 ld_sc1(__amdgpu_buffer_rsrc_t r, unsigned off) { return __builtin_amdgcn_raw_buffer_load_b128(r, off, 0, 16); }
__device__ __forceinline__ void st_sc1(u32x4 v, __amdgpu_buffer_rsrc_t r, unsigned off) { __builtin_amdgcn_raw_buffer_store_b128(v, r, off, 0, 16); }
__device__ __forceinline__ void dma16(const void* sbase, unsigned voff, unsigned lds) {
  unsigned keep;
  asm volatile("s_mov_b32 %0, m0\n\ts_mov_b32 m0, %3\n\ts_nop 0\n\tglobal_load_lds_dwordx4 %1, %2 nt\n\ts_mov_b32 m0, %0" : "=&s"(keep) : "v"(voff), "s"(sbase), "s"(lds) : "memory");
}
#define BARRIER() do { __builtin_amdgcn_sched_barrier(0); asm volatile("s_waitcnt lgkmcnt(0)\n\ts_barrier" ::: "memory"); __builtin_amdgcn_sched_barrier(0); } while (0)
#define DRAIN() asm volatile("s_waitcnt vmcnt(0)" ::: "memory")
#define NOW() ((unsigned)__builtin_amdgcn_s_memrealtime())
__device__ __forceinline__ void pace(unsigned tgt) {
  asm volatile("pace_%=:\n\ts_memrealtime s[60:61]\n\ts_waitcnt lgkmcnt(0)\n\ts_sub_u32 s60, s60, %0\n\ts_cmp_lt_i32 s60, 0\n\ts_cbranch_scc1 pace_%=" :: "s"(tgt) : "memory", "scc", "s60", "s61");
}

struct Args {
  const unsigned short *x, *kc, *vc, *wq, *wo, *w1, *w2;
  unsigned short* out;
  unsigned char* ws;
};

__global__ void __launch_bounds__(NTHR, 2) fused_decode(Args a) {
  extern __shared__ __attribute__((aligned(16))) unsigned char smem[];
  LAS unsigned char* L = (LAS unsigned char*)smem;
  const int tid = threadIdx.x, lane = tid & 63, w = __builtin_amdgcn_readfirstlane(tid >> 6);
  const int b = blockIdx.x, g = b & 7, s = b >> 3;

  if (w < 4) {
    const int hf = lane >> 5, c = lane & 31;
    const unsigned voff = lane * 16;
    const unsigned sbase = (unsigned)s * (1024u * 512u) + (unsigned)w * 2048u;
    const unsigned short* kh = a.kc + (size_t)g * SK * DH;
    const unsigned short* vh = a.vc + (size_t)g * SK * DH;
    auto soff = [&](int p) -> unsigned { return sbase + ((unsigned)((p + s) & 63) << 13); };
    u32x4 buf[W];
    BARRIER();
    auto dma_weights = [&]() {
      const unsigned short* wos = a.wo + (size_t)(g * 512 + s * 16) * D;
#pragma unroll
      for (int i = 0; i < 4; ++i) if (w * 4 + i < 14) dma16(wos, voff + (w * 4 + i) * 1024, L_WO + (w * 4 + i) * 1024);
      if (s < 28) {
        const int jf = g * 28 + s;
        const unsigned short* w2s = a.w2 + (size_t)jf * 8 * D;
        const unsigned short* w1s = a.w1 + (size_t)(jf & 7) * 56 * DFF + (jf >> 3) * 64;
#pragma unroll
        for (int i = 0; i < 2; ++i) if (w * 2 + i < 7) {
          dma16(w2s, voff + (w * 2 + i) * 1024, L_W2 + (w * 2 + i) * 1024);
          dma16(w1s, (unsigned)((w * 2 + i) * 8 + (lane >> 3)) * (DFF * 2) + (lane & 7) * 16, L_W1 + (w * 2 + i) * 1024);
        }
      }
    };
    const __amdgpu_buffer_rsrc_t rk = __builtin_amdgcn_make_buffer_rsrc((void*)kh, 0, HEADB, 0x00020000);
#pragma unroll
    for (int i = 0; i < 16; ++i) {
      if (i >= P0) asm volatile("s_waitcnt vmcnt(%0)" ::"i"(P0 - 1) : "memory");
      buf[i] = __builtin_amdgcn_raw_buffer_load_b128(rk, voff + (i & 1) * 1024, soff(i >> 1), 2);
      __builtin_amdgcn_sched_barrier(0);
    }
    DRAIN();
    dma_weights();
    BARRIER();
    const unsigned tb = __builtin_amdgcn_readfirstlane(*(const LAS unsigned*)(L + L_TM)) + (unsigned)TOFF;
#pragma unroll
    for (int i = 16; i < W; ++i) {
      if ((i & 3) == 0) pace(tb + (((i - 16) * TAU) >> 8));
      buf[i] = __builtin_amdgcn_raw_buffer_load_b128(rk, voff + (i & 1) * 1024, soff(i >> 1), 2);
      __builtin_amdgcn_sched_barrier(0);
    }
    const u32x4 q0 = *(const LAS u32x4*)(L + L_Q + c * 16), q1 = *(const LAS u32x4*)(L + L_Q + 512 + c * 16);
    LAS float* pl = (LAS float*)(L + L_P) + w * 512 + hf * 256;

    float m = -INFINITY;
    auto k_group = [&](auto slot, int j) {
      constexpr int S0 = decltype(slot)::value * 8;
      const int jn = j + RG;
      const __amdgpu_buffer_rsrc_t rn = __builtin_amdgcn_make_buffer_rsrc((void*)(jn < 16 ? kh : vh), 0, HEADB, 0x00020000);
      const int pb = (jn & 15) * 4;
      const unsigned tg = tb + (((unsigned)(j * 8 + W - 16) * TAU) >> 8);
      float x[16];
#pragma unroll
      for (int i = 0; i < 8; ++i) {
        float s0 = 0.f, s1 = 0.f;
#pragma unroll
        for (int k = 0; k < 4; ++k) { s0 = dot2(buf[S0 + i][k], q0[k], s0); s1 = dot2(buf[S0 + i][k], q1[k], s1); }
        x[2 * i] = s0; x[2 * i + 1] = s1;
        asm volatile("" : "+v"(x[2 * i]), "+v"(x[2 * i + 1]) :: "memory");
        if ((i & 3) == 0) pace(tg + ((i * TAU) >> 8));
        buf[S0 + i] = __builtin_amdgcn_raw_buffer_load_b128(rn, voff + (i & 1) * 1024, soff(pb + (i >> 1)), 2);
        __builtin_amdgcn_sched_barrier(0);
      }
#pragma unroll
      for (int h = 0; h < 8; ++h) x[h] = sw16_sum(x[h], x[h + 8]);
#pragma unroll
      for (int h = 0; h < 4; ++h) x[h] = tr_step<XOR8>(c & 8, x[h], x[h + 4]);
#pragma unroll
      for (int h = 0; h < 2; ++h) x[h] = tr_step<XOR7>(c & 4, x[h], x[h + 2]);
      x[0] = tr_step<XOR2>(c & 2, x[0], x[1]);
      x[0] += dpp<XOR1>(x[0]);
      const float sv = rbf(x[0]) * 0.0625f;
      pl[j * 16 + (c >> 1)] = sv;
      m = fmaxf(m, sv);
      __builtin_amdgcn_sched_barrier(0);
    };
#pragma unroll 1
    for (int j = 0; j < (16 / RG) * RG; j += RG) {
      k_group(IC<0>{}, j); k_group(IC<1>{}, j + 1);
      if constexpr (RG > 2) k_group(IC<2 % RG>{}, j + 2);
      if constexpr (RG > 3) k_group(IC<3 % RG>{}, j + 3);
      if (j == 0 && lane == 0) lds_inc((LAS unsigned*)(L + L_CNT) + 2);
    }
    if constexpr (16 % RG == 1) k_group(IC<0>{}, 15);

    m = fmaxf(m, dpp<ROR4>(m)); m = fmaxf(m, dpp<XOR8>(m)); m = sw16_max(m); m = sw32_max(m);
    const float nm0 = -rdlane(m, 0) * LOG2E, nm1 = -rdlane(m, 2) * LOG2E;
    if ((lane & ~2) == 0) *(LAS float*)(L + L_MLW + (w * 2 + (lane >> 1)) * 4) = m;
    if (lane == 0) lds_inc((LAS unsigned*)(L + L_CNT) + 1);

    f32x2 ac2[8];
#pragma unroll
    for (int k = 0; k < 8; ++k) ac2[k] = f32x2{0.f, 0.f};
    auto v_group = [&](auto slot, int j) {
      constexpr int S0 = decltype(slot)::value * 8;
      const int jn = j + RG;
      const bool more = jn < 32;
      const __amdgpu_buffer_rsrc_t rn = __builtin_amdgcn_make_buffer_rsrc((void*)vh, 0, more ? HEADB : 0, 0x00020000);
      const int pb = (jn & 15) * 4;
      const unsigned tg = tb + ((TGAP + (((unsigned)(j * 8 + W - 16) * TAU) >> 8)) & (more ? ~0u : 0u));
      const LAS float* pj = pl + (j - 16) * 16;
      f32x4 pp = *(const LAS f32x4*)pj;
#pragma unroll
      for (int i = 0; i < 8; i += 2) {
        const f32x4 pc = {__builtin_amdgcn_exp2f(fmaf(pp[0], LOG2E, nm0)), __builtin_amdgcn_exp2f(fmaf(pp[1], LOG2E, nm1)),
                          __builtin_amdgcn_exp2f(fmaf(pp[2], LOG2E, nm0)), __builtin_amdgcn_exp2f(fmaf(pp[3], LOG2E, nm1))};
        if (i + 2 < 8) pp = *(const LAS f32x4*)(pj + (i + 2) * 2);
#pragma unroll
        for (int u = 0; u < 2; ++u) {
          const f32x2 pa = {pc[2 * u], pc[2 * u]}, pb2 = {pc[2 * u + 1], pc[2 * u + 1]};
#pragma unroll
          for (int k = 0; k < 4; ++k) {
            const f32x2 vv = {bflo(buf[S0 + i + u][k]), bfhi(buf[S0 + i + u][k])};
            ac2[k] = __builtin_elementwise_fma(pa, vv, ac2[k]);
            ac2[4 + k] = __builtin_elementwise_fma(pb2, vv, ac2[4 + k]);
          }
          asm volatile("" : "+v"(ac2[0]), "+v"(ac2[1]), "+v"(ac2[2]), "+v"(ac2[3]), "+v"(ac2[4]), "+v"(ac2[5]), "+v"(ac2[6]), "+v"(ac2[7]) :: "memory");
          if (((i + u) & 3) == 0) pace(tg + (((i + u) * TAU) >> 8));
          buf[S0 + i + u] = __builtin_amdgcn_raw_buffer_load_b128(rn, voff + u * 1024, soff(pb + (i >> 1)), 2);
          __builtin_amdgcn_sched_barrier(0);
        }
      }
    };
    constexpr int V0 = 16 % RG;
#pragma unroll 1
    for (int j = 16; j < 16 + (16 / RG) * RG; j += RG) {
      v_group(IC<V0>{}, j); v_group(IC<(V0 + 1) % RG>{}, j + 1);
      if constexpr (RG > 2) v_group(IC<(V0 + 2) % RG>{}, j + 2);
      if constexpr (RG > 3) v_group(IC<(V0 + 3) % RG>{}, j + 3);
    }
    if constexpr (16 % RG == 1) v_group(IC<31 % RG>{}, 31);
    {
      float acc[8];
#pragma unroll
      for (int k = 0; k < 4; ++k) { acc[2 * k] = sw32_sum(ac2[k][0], ac2[4 + k][0]); acc[2 * k + 1] = sw32_sum(ac2[k][1], ac2[4 + k][1]); }
      LAS float* rec = (LAS float*)(L + L_ACC) + (w * 2 + hf) * 256 + 8 * c;
      *(LAS f32x4*)rec = f32x4{acc[0], acc[1], acc[2], acc[3]};
      *(LAS f32x4*)(rec + 4) = f32x4{acc[4], acc[5], acc[6], acc[7]};
    }
    BARRIER();
    return;
  }

  const unsigned t0h = NOW();
  const int hw = w - 4, ht = tid - 256;
  const __amdgpu_buffer_rsrc_t rws = __builtin_amdgcn_make_buffer_rsrc((void*)a.ws, 0, WS_USED, 0x00020000);
  const __amdgpu_buffer_rsrc_t rx = __builtin_amdgcn_make_buffer_rsrc((void*)a.x, 0, D * 2, 0x00020000);
  LAS unsigned* cnt = (LAS unsigned*)(L + L_CNT);
  LAS float* xn = (LAS float*)(L + L_XN) + hw * 448;
  unsigned tag;
  {
    const int cb = s >> 2, kq = s & 3;
    const __amdgpu_buffer_rsrc_t rq = __builtin_amdgcn_make_buffer_rsrc((void*)(a.wq + (size_t)kq * 112 * NQ), 0, 112 * NQ * 2, 0x00020000);
    const u32x4 xv = __builtin_amdgcn_raw_buffer_load_b128(rx, lane * 16, 0, 0);
    const int cg = ht & 7, rs = ht >> 3;
    u32x4 wv[4];
    const unsigned qo = (unsigned)rs * (NQ * 2) + (unsigned)(g * 512 + cb * 64 + cg * 8) * 2;
#pragma unroll
    for (int i = 0; i < 4; ++i) wv[i] = __builtin_amdgcn_raw_buffer_load_b128(rq, qo + i * 32 * (NQ * 2), 0, 2);
    const unsigned ep = __builtin_amdgcn_raw_buffer_load_b32(rws, 0, WS_EPOCH, 16);
    if (ht < 3) cnt[ht] = 0u;
    BARRIER();
    if (hw == 3 && lane < 8)
      dma16(a.vc + ((size_t)g * SK + (size_t)s * 1024) * DH, lane * 16, L_TLB);
    float f[8];
#pragma unroll
    for (int i = 0; i < 4; ++i) { f[2 * i] = bflo(xv[i]); f[2 * i + 1] = bfhi(xv[i]); }
    const float x0 = rdlane(f[0], 0);
    float s1 = 0.f, s2 = 0.f;
#pragma unroll
    for (int i = 0; i < 8; ++i) { const float d = f[i] - x0; s1 += d; s2 = fmaf(d, d, s2); }
    if (lane >= 56) { s1 = 0.f; s2 = 0.f; }
    s1 += dpp<XOR1>(s1); s2 += dpp<XOR1>(s2); s1 += dpp<XOR2>(s1); s2 += dpp<XOR2>(s2);
    s1 += dpp<XOR7>(s1); s2 += dpp<XOR7>(s2); s1 += dpp<XOR8>(s1); s2 += dpp<XOR8>(s2);
    { const float t = sw16_sum(s1, s2);
      const float t2 = sw32_sum(t, t); s1 = rdlane(t2, 0); s2 = rdlane(t2, 16); }
    const float md = s1 * (1.0f / D);
    const float var = fmaxf(fmaf(-md, md, s2 * (1.0f / D)), 0.f);
    const float mb = rbf(x0 + md), den = rbf(sqrtf(rbf(rbf(var) + EPS)));
    const float rden = 1.0f / den;
    if (lane < 56) {
      f32x4 n0, n1;
#pragma unroll
      for (int i = 0; i < 4; ++i) { n0[i] = rbf(rbf(f[i] - mb) * rden); n1[i] = rbf(rbf(f[4 + i] - mb) * rden); }
      *(LAS f32x4*)(xn + lane * 8) = n0; *(LAS f32x4*)(xn + lane * 8 + 4) = n1;
    }
    asm volatile("" ::: "memory");
    float xr[4];
#pragma unroll
    for (int i = 0; i < 4; ++i) { const int row = rs + 32 * i; xr[i] = xn[kq * 112 + (row < 112 ? row : 0)]; }
    float qa[8];
#pragma unroll
    for (int k = 0; k < 8; ++k) qa[k] = 0.f;
#pragma unroll
    for (int i = 0; i < 4; ++i) {
#pragma unroll
      for (int k = 0; k < 4; ++k) { qa[2 * k] = fmaf(xr[i], bflo(wv[i][k]), qa[2 * k]); qa[2 * k + 1] = fmaf(xr[i], bfhi(wv[i][k]), qa[2 * k + 1]); }
    }
    LAS float* qp = (LAS float*)(L + L_QP);
    qp[hw * 64 + cg * 8 + (lane >> 3)] = reduce8_hi(qa, lane);
    if (lane == 0) lds_inc(cnt);
    tag = __builtin_amdgcn_readfirstlane(ep) + 1u;
    st_sc1(u32x4{tag, t0h, tag, t0h}, rws, s == 0 && ht == 0 ? WS_XT + g * 128 : OOB);
    for (unsigned spin = 0; spin < SPIN_CAP; ++spin) { if (__builtin_amdgcn_readfirstlane(lds_ld(cnt)) == 4u) break; }
    asm volatile("" ::: "memory");
    const int pr = ht & 31, cbt = ht >> 5;
    f32x2 own = *(const LAS f32x2*)(qp + 2 * pr);
#pragma unroll
    for (int k = 1; k < 4; ++k) own += *(const LAS f32x2*)(qp + k * 64 + 2 * pr);
    st_sc1(granule(tag, own[0], own[1]), rws, (lane >> 3) == hw ? WS_XQ + ((g * 8 + cb) * 4 + kq) * 512 + pr * 16 : OOB);
    DRAIN();
    const u32x4 gt = ld_sc1(rws, hw == 0 && lane < 8 ? WS_XT + lane * 128 : OOB);
    u32x4 gq[4]; bool ok[4];
#pragma unroll
    for (int k = 0; k < 4; ++k) { ok[k] = cbt == cb && k == kq; gq[k] = granule(tag, own[0], own[1]); }
    for (unsigned spin = 0; spin < SPIN_CAP; ++spin) {
      asm volatile("" ::: "memory");
#pragma unroll
      for (int k = 0; k < 4; ++k) if (!ok[k]) gq[k] = ld_sc1(rws, WS_XQ + ((g * 8 + cbt) * 4 + k) * 512 + pr * 16);
#pragma unroll
      for (int k = 0; k < 4; ++k) ok[k] = tag_ok(gq[k], tag);
      if (__all(ok[0] && ok[1] && ok[2] && ok[3])) break;
      __builtin_amdgcn_s_sleep(2);
    }
    { const float q0 = (__uint_as_float(gq[0][1]) + __uint_as_float(gq[1][1])) + (__uint_as_float(gq[2][1]) + __uint_as_float(gq[3][1]));
      const float q1 = (__uint_as_float(gq[0][3]) + __uint_as_float(gq[1][3])) + (__uint_as_float(gq[2][3]) + __uint_as_float(gq[3][3]));
      *(LAS unsigned*)(L + L_Q + ht * 4) = pk2(q0, q1); }
    if (hw == 0) {
      int dm = tag_ok(gt, tag) ? min(0, (int)(gt[1] - t0h)) : 0;
      dm = min(dm, __builtin_bit_cast(int, dpp<XOR1>(__builtin_bit_cast(float, dm))));
      dm = min(dm, __builtin_bit_cast(int, dpp<XOR2>(__builtin_bit_cast(float, dm))));
      dm = min(dm, __builtin_bit_cast(int, dpp<XOR7>(__builtin_bit_cast(float, dm))));
      if (lane == 0) *(LAS unsigned*)(L + L_TM) = t0h + (unsigned)max(dm, -300);
    }
  }
  BARRIER();
  for (unsigned spin = 0; spin < SPIN_CAP; ++spin) { if (__builtin_amdgcn_readfirstlane(lds_ld(cnt + 2)) == 4u) break; __builtin_amdgcn_s_sleep(4); }
  asm volatile("" ::: "memory");
  f32x2 wo2[16];
#pragma unroll
  for (int r = 0; r < 16; ++r) {
    const unsigned v = *(const LAS unsigned*)(L + L_WO + r * (D * 2) + (ht < 224 ? ht : 0) * 4);
    wo2[r] = f32x2{bflo(v), bfhi(v)}; asm volatile("" : "+v"(wo2[r]));
  }
  unsigned xres = __builtin_amdgcn_raw_buffer_load_b32(rx, b < 224 / GP && ht < GP ? (b * GP + ht) * 4 : OOB, 0, 0);
  if (hw == 1) {
    const bool has_ffn = s < 28;
    const int j = g * 28 + s, cbh = j >> 3, e = j & 7, cg = lane & 7, rs = lane >> 3;
    float hv0 = 0.f, hv1 = 0.f;
    if (has_ffn) {
      float ha[8];
#pragma unroll
      for (int k = 0; k < 8; ++k) ha[k] = 0.f;
#pragma unroll
      for (int i = 0; i < 7; ++i) {
        const u32x4 wv = *(const LAS u32x4*)(L + L_W1 + i * 1024 + lane * 16);
        const float xr = xn[e * 56 + rs + 8 * i];
#pragma unroll
        for (int k = 0; k < 4; ++k) { ha[2 * k] = fmaf(xr, bflo(wv[k]), ha[2 * k]); ha[2 * k + 1] = fmaf(xr, bfhi(wv[k]), ha[2 * k + 1]); }
      }
      const float hs = reduce8_hi(ha, lane), hn = dpp<XOR8>(hs);
      st_sc1(granule(tag, hs, hn), rws, (lane & 8) == 0 ? WS_XH + j * 512 + (cg * 4 + (lane >> 4)) * 16 : OOB);
      DRAIN();
      pace(t0h + FFG);
      u32x4 gh = {0, 0, 0, 0}; bool ok = lane >= 32;
      for (unsigned spin = 0; spin < SPIN_CAP; ++spin) {
        asm volatile("" ::: "memory");
        if (!ok) { gh = ld_sc1(rws, WS_XH + (cbh * 8 + (lane >> 2)) * 512 + (e * 4 + (lane & 3)) * 16); ok = tag_ok(gh, tag); }
        if (__all(ok)) break;
        __builtin_amdgcn_s_sleep(16);
      }
      hv0 = lane < 32 ? __uint_as_float(gh[1]) : 0.f; hv1 = lane < 32 ? __uint_as_float(gh[3]) : 0.f;
      hv0 += xor4(hv0); hv1 += xor4(hv1); hv0 += dpp<XOR8>(hv0); hv1 += dpp<XOR8>(hv1);
      hv0 = sw16_sum(hv0, hv0); hv1 = sw16_sum(hv1, hv1);
      hv0 = fmaxf(rbf(hv0), 0.f); hv1 = fmaxf(rbf(hv1), 0.f);
    }
    float fo[8];
#pragma unroll
    for (int k = 0; k < 8; ++k) fo[k] = 0.f;
    if (has_ffn) {
#pragma unroll
      for (int r = 0; r < 8; ++r) {
        const u32x4 wv = *(const LAS u32x4*)(L + L_W2 + r * (D * 2) + (lane < 56 ? lane : 0) * 16);
        const float hr = rdlane((r & 1) ? hv1 : hv0, r >> 1);
#pragma unroll
        for (int k = 0; k < 4; ++k) { fo[2 * k] = fmaf(hr, bflo(wv[k]), fo[2 * k]); fo[2 * k + 1] = fmaf(hr, bfhi(wv[k]), fo[2 * k + 1]); }
      }
    }
    if (lane < 56) { LAS float* ff = (LAS float*)(L + L_FFN) + lane * 8;
      *(LAS f32x4*)ff = f32x4{fo[0], fo[1], fo[2], fo[3]}; *(LAS f32x4*)(ff + 4) = f32x4{fo[4], fo[5], fo[6], fo[7]}; }
  }
  if (hw == 0) {
    for (unsigned spin = 0; spin < SPIN_CAP; ++spin) { if (__builtin_amdgcn_readfirstlane(lds_ld(cnt + 1)) == 4u) break; __builtin_amdgcn_s_sleep(8); }
    asm volatile("" ::: "memory");
    const int h = lane & 1, ww = (lane >> 1) & 3;
    float l4[4];
#pragma unroll
    for (int k = 0; k < 4; ++k) {
      const float nm = -*(const LAS float*)(L + L_MLW + (k * 2 + h) * 4) * LOG2E;
      const LAS float* sc = (const LAS float*)(L + L_P) + k * 512 + lane;
      float l = 0.f;
#pragma unroll
      for (int i = 0; i < 8; ++i) l += __builtin_amdgcn_exp2f(fmaf(sc[i * 64], LOG2E, nm));
      l += dpp<XOR2>(l); l += dpp<ROR4>(l); l += dpp<XOR8>(l); l = sw16_sum(l, l); l4[k] = sw32_sum(l, l);
    }
    const f32x2 ml = {*(const LAS float*)(L + L_MLW + (ww * 2 + h) * 4), ww == 0 ? l4[0] : ww == 1 ? l4[1] : ww == 2 ? l4[2] : l4[3]};
    float M = fmaxf(ml[0], dpp<XOR2>(ml[0])); M = fmaxf(M, xor4(M));
    float Ls = __expf(ml[0] - M) * ml[1]; Ls += dpp<XOR2>(Ls); Ls += xor4(Ls);
    st_sc1(granule(tag, M, Ls), rws, lane < 2 ? WS_ML + (g * 32 + s) * 32 + lane * 16 : OOB);
    DRAIN();
    pace(t0h + MLG);
    u32x4 gm = {0, 0, 0, 0}; bool ok = false;
    for (unsigned spin = 0; spin < SPIN_CAP; ++spin) {
      asm volatile("" ::: "memory");
      if (!ok) gm = ld_sc1(rws, WS_ML + g * 1024 + lane * 16);
      ok = tag_ok(gm, tag);
      if (__all(ok)) break;
      __builtin_amdgcn_s_sleep(16);
    }
    const float ms = __uint_as_float(gm[1]);
    float Mg = fmaxf(ms, dpp<XOR2>(ms)); Mg = fmaxf(Mg, xor4(Mg)); Mg = fmaxf(Mg, dpp<XOR8>(Mg)); Mg = sw16_max(Mg); Mg = sw32_max(Mg);
    float Lg = __expf(ms - Mg) * __uint_as_float(gm[3]);
    Lg += dpp<XOR2>(Lg); Lg += xor4(Lg); Lg += dpp<XOR8>(Lg); Lg = sw16_sum(Lg, Lg); Lg = sw32_sum(Lg, Lg);
    if (lane < 8) ((LAS float*)(L + L_FAC))[h * 4 + ww] = __expf(ml[0] - Mg) / Lg;
  }
  unsigned off_x1 = WS_X1 + (g * 32 + s) * 4096 + ht * 16;
  unsigned off_g1 = WS_X1 + (g * 32 + (ht >> 3)) * 4096 + s * 128 + (ht & 7) * 16;
  unsigned off_x2 = ht < 224 ? WS_X2 + (((unsigned)ht / GP) * 256 + b) * (GP * 16) + ((unsigned)ht % GP) * 16 : OOB;
  unsigned off_g2 = WS_X2 + b * (GP * 4096) + ht * 16;
  unsigned lds_acc = L_ACC + (ht >> 7) * 1024 + (ht & 127) * 8, lds_fac = L_FAC + (ht >> 7) * 16, lds_ffn = L_FFN + (ht < 224 ? ht : 0) * 8;
  unsigned lds_cmw = L_CMB + hw * 64 + (lane & 7) * 8 + (lane >> 4) * 4, lds_cmr = L_CMB + (lane & 15) * 4;
  asm volatile("" : "+v"(xres), "+v"(off_x1), "+v"(off_g1), "+v"(off_x2), "+v"(off_g2), "+v"(lds_acc), "+v"(lds_fac), "+v"(lds_ffn), "+v"(lds_cmw), "+v"(lds_cmr));
  BARRIER();
  f32x2 o2;
  {
    const f32x4 fac = *(const LAS f32x4*)(L + lds_fac);
    const LAS float* ar = (const LAS float*)(L + lds_acc);
    const f32x2 O = __builtin_elementwise_fma(f32x2{fac[0], fac[0]}, *(const LAS f32x2*)ar, f32x2{fac[1], fac[1]} * *(const LAS f32x2*)(ar + 512)) +
                    __builtin_elementwise_fma(f32x2{fac[2], fac[2]}, *(const LAS f32x2*)(ar + 1024), f32x2{fac[3], fac[3]} * *(const LAS f32x2*)(ar + 1536));
    st_sc1(granule(tag, O[0], O[1]), rws, off_x1);
    o2 = *(const LAS f32x2*)(L + lds_ffn);
  }
  __builtin_amdgcn_s_sleep(DELAY1);
  {
    u32x4 go = {0, 0, 0, 0}; bool ok = false;
    for (unsigned spin = 0; spin < SPIN_CAP; ++spin) {
      asm volatile("" ::: "memory");
      if (!ok) go = ld_sc1(rws, off_g1);
      ok = tag_ok(go, tag);
      if (__all(ok)) break;
      __builtin_amdgcn_s_sleep(1);
    }
    float a0 = __uint_as_float(go[1]), a1 = __uint_as_float(go[3]);
    a0 += dpp<XOR8>(a0); a1 += dpp<XOR8>(a1);
    const float t = sw16_sum(a0, a1);
    const float t2 = sw32_sum(t, t);
    if ((lane & 0x28) == 0) *(LAS float*)(L + lds_cmw) = t2;
    BARRIER();
    const LAS float* cm = (const LAS float*)(L + lds_cmr);
    const float ov = rbf((cm[0] + cm[16]) + (cm[32] + cm[48]));
    f32x2 o3 = {0.f, 0.f};
#pragma unroll
    for (int r = 0; r < 8; ++r) {
      const float oa = rdlane(ov, r), ob = rdlane(ov, 8 + r);
      o2 = __builtin_elementwise_fma(f32x2{oa, oa}, wo2[r], o2); o3 = __builtin_elementwise_fma(f32x2{ob, ob}, wo2[8 + r], o3);
    }
    o2 += o3;
    st_sc1(granule(tag, o2[0], o2[1]), rws, off_x2);
  }
  if (b < 224 / GP) {
    __builtin_amdgcn_s_sleep(DELAY2);
    u32x4 gv[GP]; bool ok[GP];
#pragma unroll
    for (int k = 0; k < GP; ++k) { gv[k] = u32x4{0, 0, 0, 0}; ok[k] = false; }
    for (unsigned spin = 0; spin < SPIN_CAP; ++spin) {
      asm volatile("" ::: "memory");
      bool all = true;
#pragma unroll
      for (int k = 0; k < GP; ++k) if (!ok[k]) gv[k] = __builtin_amdgcn_raw_buffer_load_b128(rws, off_g2, k * 4096, 16);
#pragma unroll
      for (int k = 0; k < GP; ++k) { ok[k] = tag_ok(gv[k], tag); all = all && ok[k]; }
      if (__all(all)) break;
    }
    float a0 = 0.f, a1 = 0.f;
#pragma unroll
    for (int k = 0; k < GP; ++k) { a0 += __uint_as_float(gv[k][1]); a1 += __uint_as_float(gv[k][3]); }
    if (GP < 2) { a0 += dpp<XOR1>(a0); a1 += dpp<XOR1>(a1); }
    if (GP < 4) { a0 += dpp<XOR2>(a0); a1 += dpp<XOR2>(a1); }
    if (GP < 8) { a0 += xor4(a0); a1 += xor4(a1); }
    a0 += dpp<XOR8>(a0); a1 += dpp<XOR8>(a1);
    const float t = sw16_sum(a0, a1), t2 = sw32_sum(t, t);
    LAS float* fin = (LAS float*)(L + L_FIN);
    if ((lane & 0x2f) < GP) fin[hw * 16 + 2 * (lane & 7) + (lane >> 4)] = t2;
    BARRIER();
    if (ht < GP) {
      const LAS f32x2* f2 = (const LAS f32x2*)fin + ht;
      const f32x2 r = f32x2{bflo(xres), bfhi(xres)} + ((f2[0] + f2[8]) + (f2[16] + f2[24]));
      __builtin_amdgcn_raw_buffer_store_b32(pk2(r[0], r[1]), __builtin_amdgcn_make_buffer_rsrc((void*)a.out, 0, D * 2, 0x00020000), (b * GP + ht) * 4, 0, 16);
    }
    __builtin_amdgcn_raw_buffer_store_b32(tag, rws, b == 0 && ht == 0 ? WS_EPOCH : OOB, 0, 16);
  }
}

extern "C" void kernel_launch(void* const* d_in, const int* in_sizes, int n_in, void* d_out, int out_size, void* d_ws, size_t ws_size, hipStream_t stream) {
  Args a;
  a.x = (const unsigned short*)d_in[0]; a.kc = (const unsigned short*)d_in[1]; a.vc = (const unsigned short*)d_in[2];
  a.wq = (const unsigned short*)d_in[3]; a.wo = (const unsigned short*)d_in[4]; a.w1 = (const unsigned short*)d_in[6]; a.w2 = (const unsigned short*)d_in[7];
  a.out = (unsigned short*)d_out; a.ws = (unsigned char*)d_ws;
  (void)hipFuncSetAttribute((const void*)fused_decode, hipFuncAttributeMaxDynamicSharedMemorySize, LDS_BYTES);
  hipLaunchKernelGGL(fused_decode, dim3(NWG), dim3(NTHR), LDS_BYTES, stream, a);
}
```
